```python
import torch

def _softmax(x, axis=-1):
    return torch.softmax(x, dim=axis)
def _gelu(x):
    return torch.nn.functional.gelu(x, approximate="tanh")
def _silu(x):
    return torch.nn.functional.silu(x)
def _sigmoid(x):
    return torch.sigmoid(x)
def _layernorm(x, axis=-1, eps=1e-5):
    m = x.mean(dim=axis, keepdim=True)
    v = x.var(dim=axis, keepdim=True, unbiased=False)
    return (x - m) / torch.sqrt(v + eps)
def _scatter_add(tgt, idx, src, axis=0):
    return tgt.clone().index_add_(axis, idx, src)
def _segment_sum(src, seg, n):
    out = torch.zeros((n,) + src.shape[1:], dtype=src.dtype)
    return out.index_add_(0, seg, src)
def _pad(x, pw):
    flat = tuple(v for pair in reversed(pw) for v in pair)
    return torch.nn.functional.pad(x, flat)

kernel_name = 'standalone__deep_cross_wide_deep__recsys'

def setup_inputs():

    _w = (lambda a: (lambda t: t.to(torch.bfloat16) if t.is_floating_point() else t)(torch.as_tensor(a)))
    B, F, D = 4096, 26, 128
    V, Nd = min(1000, 100000), 13
    Din = F * D
    H = 4 * D
    return {
        "idx": _w((lambda lo, hi, *s: torch.randint(lo, hi, s))(0, V, B, F).to(torch.int64)),
        "table": _w((lambda *s: torch.randn(*s, dtype=torch.bfloat16) * 1.6)(V, D) * 0.05),
        "x_wide": _w((lambda *s: torch.randn(*s, dtype=torch.bfloat16) * 1.6)(B, Nd)),
        "w_wide": _w((lambda *s: torch.randn(*s, dtype=torch.bfloat16) * 1.6)(Nd) * 0.05),
        "b_wide": _w((lambda *s: torch.randn(*s, dtype=torch.bfloat16) * 1.6)(1) * 0.01),
        "W1": _w((lambda *s: torch.randn(*s, dtype=torch.bfloat16) * 1.6)(Din, H) * (2.0 / Din) ** 0.5),
        "b1": _w((lambda *s: torch.randn(*s, dtype=torch.bfloat16) * 1.6)(H) * 0.01),
        "W2": _w((lambda *s: torch.randn(*s, dtype=torch.bfloat16) * 1.6)(H) * (1.0 / H) ** 0.5),
        "b2": _w((lambda *s: torch.randn(*s, dtype=torch.bfloat16) * 1.6)(1) * 0.01),
    }

def reference(idx, table, x_wide, w_wide, b_wide, W1, b1, W2, b2):
    emb = table[idx]
    deep_in = emb.reshape(emb.shape[0], -1)
    h = deep_in @ W1 + b1
    h = h * (lambda x, dt: x.to(dt))(h > 0, h.dtype)
    deep_logit = h @ W2 + b2
    wide_logit = x_wide @ w_wide + b_wide
    return _sigmoid(wide_logit + deep_logit)
```

```cpp
#include <hip/hip_runtime.h>
#include <hip/hip_bf16.h>

#define GAS __attribute__((address_space(1)))
#define LAS __attribute__((address_space(3)))
typedef __attribute__((ext_vector_type(8))) short bf16x8;
typedef __attribute__((ext_vector_type(4))) float f32x4;
typedef __attribute__((ext_vector_type(4))) unsigned u32x4;
typedef __attribute__((ext_vector_type(2))) unsigned u32x2;
typedef GAS unsigned gu32;
typedef GAS unsigned long long gu64;

constexpr int NB = 4096, NF = 26, ND = 128, NV = 1000, NH = 512, NWIDE = 13;
constexpr int BM = 128, BN = 64;
constexpr int TBL_BYTES = NV * 128;
constexpr int LDS_BYTES = TBL_BYTES + 1024;
constexpr int WS_GRAN_OFF = 8192;
constexpr unsigned TAGB = 0x9E3779B9u;

__device__ __forceinline__ float bf2f(unsigned short h) { return __uint_as_float((unsigned)h << 16); }
__device__ __forceinline__ float rbf(float x) {
  unsigned u = __float_as_uint(x);
  if ((u & 0x7fffffffu) > 0x7f800000u) return x;
  u += 0x7fffu + ((u >> 16) & 1u);
  return __uint_as_float(u & 0xffff0000u);
}

struct Raw { u32x4 v[8]; };
struct Frag { u32x4 x[4], y[4]; };

__device__ __forceinline__ void load_raw(Raw& r, const char* p) {
#pragma unroll
  for (int j = 0; j < 8; ++j) r.v[j] = *(const GAS u32x4*)(p + j * 1024);
}
__device__ __forceinline__ void shuffle_t(Frag& f, const Raw& r, int t, unsigned selA, unsigned selB) {
#pragma unroll
  for (int p = 0; p < 4; ++p) {
    unsigned r0 = r.v[2 * p][t], r1 = r.v[2 * p + 1][t];
    unsigned pa = __builtin_amdgcn_perm(r1, r0, selA);
    unsigned pb = __builtin_amdgcn_perm(r1, r0, selB);
    f.x[t][p] = __builtin_amdgcn_update_dpp(pa, pb, 0x128, 0xF, 0xC, false);
    f.y[t][p] = __builtin_amdgcn_update_dpp(pa, pb, 0x128, 0xF, 0x3, false);
  }
}

template <int NT>
__device__ __forceinline__ void lds_read(u32x4 (&a)[8], const char* lds, const unsigned* addr, unsigned x) {
#pragma unroll
  for (int i = 0; i < NT; ++i) a[i] = *(const LAS u32x4*)(LAS char*)(size_t)(addr[i] ^ x);
}
template <int NT>
__device__ __forceinline__ void mma(f32x4 (&acc)[8][4], const u32x4 (&w)[4], const u32x4 (&a)[8]) {
#pragma unroll
  for (int i = 0; i < NT; ++i)
#pragma unroll
    for (int t = 0; t < 4; ++t)
      acc[i][t] = __builtin_amdgcn_mfma_f32_16x16x32_bf16(__builtin_bit_cast(bf16x8, w[t]), __builtin_bit_cast(bf16x8, a[i]), acc[i][t], 0, 0, 0);
}

__device__ __forceinline__ void dma_table(const char* table, char* lds, int w, int lane, int ph) {
  const int r0 = w * 250 + (lane >> 3);
  const char* src = table + r0 * 256 + ph * 128 + (((lane & 7) ^ (r0 & 7)) << 4);
  char* dst = lds + w * 32000;
#pragma unroll
  for (int i = 0; i < 31; ++i)
    __builtin_amdgcn_global_load_lds((const GAS unsigned*)(src + i * 2048), (LAS unsigned*)(dst + i * 1024), 16, 0, 0);
  if (lane < 16)
    __builtin_amdgcn_global_load_lds((const GAS unsigned*)(src + 31 * 2048), (LAS unsigned*)(dst + 31 * 1024), 16, 0, 0);
}

__global__ __launch_bounds__(256, 1) void wide_deep_kernel(
    const int* __restrict__ idx, const char* __restrict__ table, const unsigned short* __restrict__ xw,
    const unsigned short* __restrict__ ww, const unsigned short* __restrict__ bw, const char* __restrict__ W1,
    const unsigned short* __restrict__ b1, const unsigned short* __restrict__ W2,
    const unsigned short* __restrict__ b2, unsigned short* __restrict__ out, char* ws) {
  extern __shared__ __attribute__((aligned(16))) char lds[];
  const int lane = threadIdx.x & 63;
  const int w = __builtin_amdgcn_readfirstlane(threadIdx.x >> 6);
  const int ns = blockIdx.x & 7, rb = blockIdx.x >> 3;
  const int la = lane >> 4, lb = lane & 15;

  const unsigned epoch = __hip_atomic_load((gu32*)(ws + rb * 256), __ATOMIC_RELAXED, __HIP_MEMORY_SCOPE_AGENT);
  u32x4 ia[8]; u32x2 ib[8]; int ih[4];
  {
    const char* ip = (const char*)idx + (size_t)(rb * BM + lb) * (NF * 4);
#pragma unroll
    for (int i = 0; i < 8; ++i) {
      const char* q = ip + ((i + 2 * w) & 7) * (16 * NF * 4);
      ia[i] = *(const GAS u32x4 __attribute__((aligned(8)))*)(q + w * 24);
      ib[i] = *(const GAS u32x2*)(q + w * 24 + 16);
      if (i < 4) ih[i] = *(const GAS int*)(q + (24 + (w & 1)) * 4);
    }
  }
  dma_table(table, lds, w, lane, 0);

  const int g = la + ((lb >> 3) << 2);
  const char* wp = W1 + (size_t)(g * 8) * 1024 + ns * 128 + (lb & 7) * 16;
  const int fh = 24 + (w & 1);
  auto blk = [&](int ph, int n) -> const char* {
    int f = n < 6 ? w * 6 + n : fh;
    return wp + (size_t)(f * 128 + ph * 64) * 1024;
  };
  Raw raw0, raw1;
  load_raw(raw0, blk(0, 0));
  load_raw(raw1, blk(0, 1));

  unsigned addr[6][8], addrh[4];
  {
    auto mk = [&](unsigned r) -> unsigned {
      r = r < (unsigned)NV ? r : (unsigned)(NV - 1);
      return r * 128u + (((unsigned)la ^ (r & 7u)) << 4);
    };
#pragma unroll
    for (int i = 0; i < 8; ++i) {
      addr[0][i] = mk(ia[i][0]); addr[1][i] = mk(ia[i][1]); addr[2][i] = mk(ia[i][2]); addr[3][i] = mk(ia[i][3]);
      addr[4][i] = mk(ib[i][0]); addr[5][i] = mk(ib[i][1]);
      if (i < 4) addrh[i] = mk((unsigned)ih[i]);
    }
  }
  const unsigned selA = lb < 8 ? 0x05040100u : 0x07060302u;
  const unsigned selB = lb < 8 ? 0x07060302u : 0x05040100u;

  f32x4 acc[8][4];
#pragma unroll
  for (int i = 0; i < 8; ++i)
#pragma unroll
    for (int t = 0; t < 4; ++t) acc[i][t] = f32x4{0.f, 0.f, 0.f, 0.f};

  Frag fr0, fr1;
#pragma unroll
  for (int t = 0; t < 4; ++t) shuffle_t(fr0, raw0, t, selA, selB);
  asm volatile("s_waitcnt vmcnt(0)" ::: "memory");
  __builtin_amdgcn_s_barrier();

  u32x4 a0[8], a1[8];
  for (int ph = 0; ph < 2; ++ph) {
    lds_read<8>(a0, lds, addr[0], 0);
#define BLOCK(N, FR_CUR, FR_NXT, RAW_NXT, RAW_LD, LDP, ADDR_CUR, ADDR_NXT, NT_NXT)   \
    {                                                                                  \
      load_raw(RAW_LD, LDP);                                                           \
      lds_read<8>(a1, lds, ADDR_CUR, 64);                                              \
      shuffle_t(FR_NXT, RAW_NXT, 0, selA, selB);                                       \
      shuffle_t(FR_NXT, RAW_NXT, 1, selA, selB);                                       \
      mma<8>(acc, FR_CUR.x, a0);                                                       \
      lds_read<NT_NXT>(a0, lds, ADDR_NXT, 0);                                          \
      shuffle_t(FR_NXT, RAW_NXT, 2, selA, selB);                                       \
      shuffle_t(FR_NXT, RAW_NXT, 3, selA, selB);                                       \
      mma<8>(acc, FR_CUR.y, a1);                                                       \
    }
    BLOCK(0, fr0, fr1, raw1, raw0, blk(ph, 2), addr[0], addr[1], 8)
    BLOCK(1, fr1, fr0, raw0, raw1, blk(ph, 3), addr[1], addr[2], 8)
    BLOCK(2, fr0, fr1, raw1, raw0, blk(ph, 4), addr[2], addr[3], 8)
    BLOCK(3, fr1, fr0, raw0, raw1, blk(ph, 5), addr[3], addr[4], 8)
    BLOCK(4, fr0, fr1, raw1, raw0, blk(ph, 6), addr[4], addr[5], 8)
    BLOCK(5, fr1, fr0, raw0, raw1, blk(1, 0), addr[5], addrh, 4)
    {
      load_raw(raw0, blk(1, 1));
      lds_read<4>(a1, lds, addrh, 64);
      shuffle_t(fr1, raw1, 0, selA, selB);
      shuffle_t(fr1, raw1, 1, selA, selB);
      mma<4>(acc, fr0.x, a0);
      shuffle_t(fr1, raw1, 2, selA, selB);
      shuffle_t(fr1, raw1, 3, selA, selB);
      mma<4>(acc, fr0.y, a1);
    }
    if (ph == 0) {
      fr0 = fr1;
      raw1 = raw0;
      asm volatile("s_waitcnt lgkmcnt(0)" ::: "memory");
      __builtin_amdgcn_s_barrier();
      dma_table(table, lds, w, lane, 1);
      asm volatile("s_waitcnt vmcnt(0)" ::: "memory");
      __builtin_amdgcn_s_barrier();
    }
  }

  asm volatile("s_waitcnt lgkmcnt(0)" ::: "memory");
  __builtin_amdgcn_s_barrier();
  {
    LAS char* base = (LAS char*)lds + lane * 16;
#pragma unroll
    for (int i = 2; i < 8; ++i) {
      const int dw = ((i >> 1) + w) & 3;
#pragma unroll
      for (int t = 0; t < 4; ++t)
        *(LAS f32x4*)(base + (((dw * 3 + ((i >> 1) - 1)) * 2 + (i & 1)) * 4 + t) * 1024) = acc[i][t];
    }
    asm volatile("s_waitcnt lgkmcnt(0)" ::: "memory");
    __builtin_amdgcn_s_barrier();
#pragma unroll
    for (int s = 0; s < 3; ++s)
#pragma unroll
      for (int i = 0; i < 2; ++i)
#pragma unroll
        for (int t = 0; t < 4; ++t) acc[i][t] += *(const LAS f32x4*)(base + (((w * 3 + s) * 2 + i) * 4 + t) * 1024);
  }
  float part[2] = {0.f, 0.f};
#pragma unroll
  for (int t = 0; t < 4; ++t)
#pragma unroll
    for (int r = 0; r < 4; ++r) {
      const int m = 4 * la + r;
      const int n = ns * BN + 8 * (m & 7) + 2 * t + (m >> 3);
      const float bb = bf2f(b1[n]), w2 = bf2f(W2[n]);
#pragma unroll
      for (int i = 0; i < 2; ++i) {
        float h = rbf(rbf(acc[i][t][r]) + bb);
        h = h > 0.f ? h : h * 0.f;
        part[i] += h * w2;
      }
    }
#pragma unroll
  for (int i = 0; i < 2; ++i) {
    part[i] += __shfl_xor(part[i], 16);
    part[i] += __shfl_xor(part[i], 32);
  }
  const unsigned tag = epoch + 1u;
  gu64* gran = (gu64*)(ws + WS_GRAN_OFF) + (size_t)rb * (BM * 16);
  {
    const int tile = la >> 1, half = la & 1;
    const float v = tile ? part[1] : part[0];
    const int row = (2 * w + tile) * 16 + lb;
    const unsigned tg = half ? (tag ^ TAGB) : tag;
    __hip_atomic_store(gran + row * 16 + ns * 2 + half, ((unsigned long long)tg << 32) | __float_as_uint(v),
                       __ATOMIC_RELAXED, __HIP_MEMORY_SCOPE_AGENT);
  }
  if (w != 0) return;
  const int frow = rb * BM + ns * 16 + (lane >> 2), fq = lane & 3;
  float wide = 0.f;
#pragma unroll
  for (int e = 0; e < 4; ++e) {
    const int c = fq + 4 * e;
    if (c < NWIDE) wide += bf2f(xw[frow * NWIDE + c]) * bf2f(ww[c]);
  }
  wide += __shfl_xor(wide, 1);
  wide += __shfl_xor(wide, 2);
  wide = rbf(rbf(wide) + bf2f(bw[0]));
  const float bias2 = bf2f(b2[0]);
  gu64* gp = gran + ns * 256 + lane * 4;
  float s = 0.f;
  for (unsigned spins = 0; spins < (1u << 22); ++spins) {
    unsigned long long x0 = __hip_atomic_load(gp + 0, __ATOMIC_RELAXED, __HIP_MEMORY_SCOPE_AGENT);
    unsigned long long x1 = __hip_atomic_load(gp + 1, __ATOMIC_RELAXED, __HIP_MEMORY_SCOPE_AGENT);
    unsigned long long x2 = __hip_atomic_load(gp + 2, __ATOMIC_RELAXED, __HIP_MEMORY_SCOPE_AGENT);
    unsigned long long x3 = __hip_atomic_load(gp + 3, __ATOMIC_RELAXED, __HIP_MEMORY_SCOPE_AGENT);
    const bool ok = (unsigned)(x0 >> 32) == tag && (unsigned)(x1 >> 32) == (tag ^ TAGB) &&
                    (unsigned)(x2 >> 32) == tag && (unsigned)(x3 >> 32) == (tag ^ TAGB);
    s = __uint_as_float((unsigned)x0) + __uint_as_float((unsigned)x2);
    if (__all(ok)) break;
    __builtin_amdgcn_s_sleep(1);
  }
  s += __shfl_xor(s, 1);
  s += __shfl_xor(s, 2);
  const float deep = rbf(rbf(s) + bias2);
  const float z = rbf(wide + deep);
  const float y = rbf(1.f / (1.f + __expf(-z)));
  if (fq == 0) out[frow] = (unsigned short)(__float_as_uint(y) >> 16);
  if (ns == 0 && lane == 0)
    __hip_atomic_store((gu32*)(ws + rb * 256), tag, __ATOMIC_RELAXED, __HIP_MEMORY_SCOPE_AGENT);
}

extern "C" void kernel_launch(void* const* d_in, const int* in_sizes, int n_in, void* d_out, int out_size,
                              void* d_ws, size_t ws_size, hipStream_t stream) {
  (void)in_sizes; (void)n_in; (void)out_size; (void)ws_size;
  (void)hipFuncSetAttribute((const void*)wide_deep_kernel, hipFuncAttributeMaxDynamicSharedMemorySize, LDS_BYTES);
  wide_deep_kernel<<<256, 256, LDS_BYTES, stream>>>(
      (const int*)d_in[0], (const char*)d_in[1], (const unsigned short*)d_in[2], (const unsigned short*)d_in[3],
      (const unsigned short*)d_in[4], (const char*)d_in[5], (const unsigned short*)d_in[6],
      (const unsigned short*)d_in[7], (const unsigned short*)d_in[8], (unsigned short*)d_out, (char*)d_ws);
}
```

```cpp
#include <hip/hip_runtime.h>
#include <hip/hip_bf16.h>

#define GAS __attribute__((address_space(1)))
#define LAS __attribute__((address_space(3)))
typedef __attribute__((ext_vector_type(4))) float f32x4;
typedef __attribute__((ext_vector_type(4))) unsigned u32x4;
typedef __attribute__((ext_vector_type(2))) float f32x2;
typedef __attribute__((ext_vector_type(2))) __bf16 bf16x2;
typedef __attribute__((ext_vector_type(2))) short s16x2;
typedef GAS unsigned gu32;
typedef GAS unsigned long long gu64;

constexpr int NWIDE = 13;
constexpr int BM = 128, BN = 64;
constexpr int LDS_SMALL = 141312;
constexpr int LDS_BYTES = LDS_SMALL + 256;
constexpr int WS_GRAN_OFF = 8192;
constexpr unsigned TAGB = 0x9E3779B9u;

__device__ __forceinline__ float bf2f(unsigned short h) { return __uint_as_float((unsigned)h << 16); }
__device__ __forceinline__ float rbf(float x) {
  unsigned u = __float_as_uint(x);
  if ((u & 0x7fffffffu) > 0x7f800000u) return x;
  u += 0x7fffu + ((u >> 16) & 1u);
  return __uint_as_float(u & 0xffff0000u);
}

__global__ __launch_bounds__(256, 1) void wide_deep_kernel(
    const int* __restrict__ idx, const char* __restrict__ table, const unsigned short* __restrict__ xw,
    const unsigned short* __restrict__ ww, const unsigned short* __restrict__ bw, const char* __restrict__ W1,
    const unsigned short* __restrict__ b1, const unsigned short* __restrict__ W2,
    const unsigned short* __restrict__ b2, unsigned short* __restrict__ out, char* ws) {
  extern __shared__ __attribute__((aligned(16))) char lds[];
  const int lane = threadIdx.x & 63;
  const int w = __builtin_amdgcn_readfirstlane(threadIdx.x >> 6);
  const int ns = blockIdx.x & 7, rb = blockIdx.x >> 3;
  const int la = lane >> 4, lb = lane & 15;

  const unsigned epoch = __hip_atomic_load((gu32*)(ws + rb * 256), __ATOMIC_RELAXED, __HIP_MEMORY_SCOPE_AGENT);
  const int frow = rb * BM + ns * 16 + (lane >> 2), fq = lane & 3;
  unsigned short xv[4], wv[4];
#pragma unroll
  for (int e = 0; e < 4; ++e) {
    const int c = fq + 4 * e, cc = c < NWIDE ? c : 0;
    xv[e] = xw[frow * NWIDE + cc];
    wv[e] = ww[cc];
  }
  const unsigned short bwv = bw[0], b2v = b2[0];
  f32x4 acc[2][4];
  {
    const unsigned long long pi = (unsigned long long)idx, pt = (unsigned long long)table, pw = (unsigned long long)W1;
    asm volatile(
      "0:\n"
      "s_mov_b32 s48, 0\n"
      "v_mbcnt_lo_u32_b32 v238, -1, 0\n"
      "v_mbcnt_hi_u32_b32 v238, -1, v238\n"
      "s_mul_i32 s49, %[rb], 13312\n"
      "s_lshl_b32 s52, %[w], 10\n"
      "s_add_u32 s49, s49, s52\n"
      "s_add_u32 s50, %[idx0], s49\n"
      "s_addc_u32 s51, %[idx1], 0\n"
      "s_add_u32 m0, s52, 128000\n"
      "v_lshlrev_b32 v232, 4, v238\n"
      "global_load_lds_dwordx4 v232, s[50:51]\n"
      "s_add_u32 m0, m0, 4096\n"
      "s_add_u32 s50, s50, 4096\n"
      "s_addc_u32 s51, s51, 0\n"
      "global_load_lds_dwordx4 v232, s[50:51]\n"
      "s_add_u32 m0, m0, 4096\n"
      "s_add_u32 s50, s50, 4096\n"
      "s_addc_u32 s51, s51, 0\n"
      "global_load_lds_dwordx4 v232, s[50:51]\n"
      "s_add_u32 m0, m0, 4096\n"
      "s_add_u32 s50, s50, 4096\n"
      "s_addc_u32 s51, s51, 0\n"
      "s_cmp_lg_u32 %[w], 0\n"
      "s_cbranch_scc1 6f\n"
      "global_load_lds_dwordx4 v232, s[50:51]\n"
      "6:\n"
      "s_lshl_b32 s52, %[rb], 2\n"
      "s_add_u32 s52, s52, %[w]\n"
      "s_lshl_b32 s52, s52, 4\n"
      "v_and_b32 v232, 15, v238\n"
      "v_add_u32 v232, s52, v232\n"
      "s_movk_i32 s54, 1999\n"
      "v_min_u32 v232, s54, v232\n"
      "v_lshlrev_b32 v232, 7, v232\n"
      "global_load_dword a193, v232, %[tab]\n"
      "s_mul_i32 s52, %[rb], 104\n"
      "s_mul_i32 s53, %[w], 26\n"
      "s_add_u32 s52, s52, s53\n"
      "v_min_u32 v232, 25, v238\n"
      "v_add_u32 v232, s52, v232\n"
      "v_lshlrev_b32 v232, 10, v232\n"
      "s_lshl_b32 s52, %[ns], 7\n"
      "v_add_u32 v232, s52, v232\n"
      "global_load_dword a192, v232, %[w1]\n"
      "v_lshrrev_b32 v237, 4, v238\n"
      "v_and_b32 v239, 15, v238\n"
      "v_lshrrev_b32 v232, 3, v239\n"
      "v_lshl_add_u32 v232, v232, 2, v237\n"
      "v_lshlrev_b32 v232, 13, v232\n"
      "v_and_b32 v233, 7, v239\n"
      "v_lshl_add_u32 v234, v233, 4, v232\n"
      "s_lshl_b32 s49, %[ns], 7\n"
      "s_add_u32 s49, s49, 4096\n"
      "s_mul_i32 s52, %[w], 786432\n"
      "s_add_u32 s52, s52, s49\n"
      "s_add_u32 s40, %[w10], s52\n"
      "s_addc_u32 s41, %[w11], 0\n"
      "s_add_u32 s46, s40, 65536\n"
      "s_addc_u32 s47, s41, 0\n"
      "s_and_b32 s52, %[w], 1\n"
      "s_add_u32 s52, s52, 24\n"
      "s_lshl_b32 s52, s52, 17\n"
      "s_add_u32 s52, s52, s49\n"
      "s_add_u32 s44, %[w10], s52\n"
      "s_addc_u32 s45, %[w11], 0\n"
      "global_load_dwordx4 v[32:35], v234, s[40:41] offset:-4096\n"
      "global_load_dwordx4 v[36:39], v234, s[40:41] offset:-3072\n"
      "global_load_dwordx4 v[40:43], v234, s[40:41] offset:-2048\n"
      "global_load_dwordx4 v[44:47], v234, s[40:41] offset:-1024\n"
      "global_load_dwordx4 v[48:51], v234, s[40:41] offset:0\n"
      "global_load_dwordx4 v[52:55], v234, s[40:41] offset:1024\n"
      "global_load_dwordx4 v[56:59], v234, s[40:41] offset:2048\n"
      "global_load_dwordx4 v[60:63], v234, s[40:41] offset:3072\n"
      "s_getpc_b64 s[58:59]\n"
      "7:\n"
      "s_lshl_b32 s52, %[w], 13\n"
      "v_lshlrev_b32 v232, 7, v238\n"
      "v_add_u32 v232, s52, v232\n"
      "s_mov_b32 s52, 9f-7b+1536\n"
      "v_min_u32 v232, s52, v232\n"
      "global_load_dword a194, v232, s[58:59]\n"
      "v_mov_b32 v232, 0\n"
      "global_load_dword a195, v232, %[gr]\n"
      "global_load_dword a196, v232, %[op]\n"
      "s_lshl_b32 s52, %[ns], 7\n"
      "v_and_b32 v232, 7, v238\n"
      "v_lshlrev_b32 v232, 4, v232\n"
      "v_add_u32 v232, s52, v232\n"
      "s_mov_b64 exec, 0xff\n"
      "s_mov_b32 m0, 141312\n"
      "s_nop 0\n"
      "global_load_lds_dwordx4 v232, %[b1]\n"
      "s_mov_b32 m0, 141440\n"
      "s_nop 0\n"
      "global_load_lds_dwordx4 v232, %[w2]\n"
      "s_mov_b64 exec, -1\n"
      "s_movk_i32 s54, 104\n"
      "v_mul_u32_u24 v91, s54, v239\n"
      "s_mov_b32 vcc_lo, 0xff00ff00\n"
      "s_mov_b32 vcc_hi, 0xff00ff00\n"
      "v_mov_b32 v232, 0x05040100\n"
      "v_mov_b32 v233, 0x07060302\n"
      "v_cndmask_b32 v235, v232, v233, vcc\n"
      "v_cndmask_b32 v236, v233, v232, vcc\n"
      "s_mul_i32 s52, %[w], 250\n"
      "v_lshrrev_b32 v232, 3, v238\n"
      "v_add_u32 v233, s52, v232\n"
      "v_xor_b32 v233, v233, v238\n"
      "v_and_b32 v233, 7, v233\n"
      "v_lshlrev_b32 v233, 4, v233\n"
      "v_lshl_add_u32 v239, v232, 8, v233\n"
      "s_mul_i32 s56, %[w], 64000\n"
      "s_mul_i32 s57, %[w], 32000\n"
      "s_add_u32 s49, s56, 0\n"
      "s_add_u32 s42, %[tab0], s49\n"
      "s_addc_u32 s43, %[tab1], 0\n"
      "s_mov_b32 m0, s57\n"
      "s_nop 0\n"
      "global_load_lds_dwordx4 v239, s[42:43]\n"
      "s_add_u32 m0, m0, 1024\n"
      "s_add_u32 s42, s42, 2048\n"
      "s_addc_u32 s43, s43, 0\n"
      "v_accvgpr_write_b32 a0, 0\n"
      "v_accvgpr_write_b32 a1, 0\n"
      "v_accvgpr_write_b32 a2, 0\n"
      "v_accvgpr_write_b32 a3, 0\n"
      "v_accvgpr_write_b32 a4, 0\n"
      "v_accvgpr_write_b32 a5, 0\n"
      "v_accvgpr_write_b32 a6, 0\n"
      "v_accvgpr_write_b32 a7, 0\n"
      "global_load_lds_dwordx4 v239, s[42:43]\n"
      "s_add_u32 m0, m0, 1024\n"
      "s_add_u32 s42, s42, 2048\n"
      "s_addc_u32 s43, s43, 0\n"
      "v_accvgpr_write_b32 a8, 0\n"
      "v_accvgpr_write_b32 a9, 0\n"
      "v_accvgpr_write_b32 a10, 0\n"
      "v_accvgpr_write_b32 a11, 0\n"
      "v_accvgpr_write_b32 a12, 0\n"
      "v_accvgpr_write_b32 a13, 0\n"
      "v_accvgpr_write_b32 a14, 0\n"
      "v_accvgpr_write_b32 a15, 0\n"
      "global_load_lds_dwordx4 v239, s[42:43]\n"
      "s_add_u32 m0, m0, 1024\n"
      "s_add_u32 s42, s42, 2048\n"
      "s_addc_u32 s43, s43, 0\n"
      "v_accvgpr_write_b32 a16, 0\n"
      "v_accvgpr_write_b32 a17, 0\n"
      "v_accvgpr_write_b32 a18, 0\n"
      "v_accvgpr_write_b32 a19, 0\n"
      "v_accvgpr_write_b32 a20, 0\n"
      "v_accvgpr_write_b32 a21, 0\n"
      "v_accvgpr_write_b32 a22, 0\n"
      "v_accvgpr_write_b32 a23, 0\n"
      "global_load_lds_dwordx4 v239, s[42:43]\n"
      "s_add_u32 m0, m0, 1024\n"
      "s_add_u32 s42, s42, 2048\n"
      "s_addc_u32 s43, s43, 0\n"
      "v_accvgpr_write_b32 a24, 0\n"
      "v_accvgpr_write_b32 a25, 0\n"
      "v_accvgpr_write_b32 a26, 0\n"
      "v_accvgpr_write_b32 a27, 0\n"
      "v_accvgpr_write_b32 a28, 0\n"
      "v_accvgpr_write_b32 a29, 0\n"
      "v_accvgpr_write_b32 a30, 0\n"
      "v_accvgpr_write_b32 a31, 0\n"
      "global_load_lds_dwordx4 v239, s[42:43]\n"
      "s_add_u32 m0, m0, 1024\n"
      "s_add_u32 s42, s42, 2048\n"
      "s_addc_u32 s43, s43, 0\n"
      "v_accvgpr_write_b32 a32, 0\n"
      "v_accvgpr_write_b32 a33, 0\n"
      "v_accvgpr_write_b32 a34, 0\n"
      "v_accvgpr_write_b32 a35, 0\n"
      "v_accvgpr_write_b32 a36, 0\n"
      "v_accvgpr_write_b32 a37, 0\n"
      "v_accvgpr_write_b32 a38, 0\n"
      "v_accvgpr_write_b32 a39, 0\n"
      "global_load_lds_dwordx4 v239, s[42:43]\n"
      "s_add_u32 m0, m0, 1024\n"
      "s_add_u32 s42, s42, 2048\n"
      "s_addc_u32 s43, s43, 0\n"
      "v_accvgpr_write_b32 a40, 0\n"
      "v_accvgpr_write_b32 a41, 0\n"
      "v_accvgpr_write_b32 a42, 0\n"
      "v_accvgpr_write_b32 a43, 0\n"
      "v_accvgpr_write_b32 a44, 0\n"
      "v_accvgpr_write_b32 a45, 0\n"
      "v_accvgpr_write_b32 a46, 0\n"
      "v_accvgpr_write_b32 a47, 0\n"
      "global_load_lds_dwordx4 v239, s[42:43]\n"
      "s_add_u32 m0, m0, 1024\n"
      "s_add_u32 s42, s42, 2048\n"
      "s_addc_u32 s43, s43, 0\n"
      "v_accvgpr_write_b32 a48, 0\n"
      "v_accvgpr_write_b32 a49, 0\n"
      "v_accvgpr_write_b32 a50, 0\n"
      "v_accvgpr_write_b32 a51, 0\n"
      "v_accvgpr_write_b32 a52, 0\n"
      "v_accvgpr_write_b32 a53, 0\n"
      "v_accvgpr_write_b32 a54, 0\n"
      "v_accvgpr_write_b32 a55, 0\n"
      "global_load_lds_dwordx4 v239, s[42:43]\n"
      "s_add_u32 m0, m0, 1024\n"
      "s_add_u32 s42, s42, 2048\n"
      "s_addc_u32 s43, s43, 0\n"
      "v_accvgpr_write_b32 a56, 0\n"
      "v_accvgpr_write_b32 a57, 0\n"
      "v_accvgpr_write_b32 a58, 0\n"
      "v_accvgpr_write_b32 a59, 0\n"
      "v_accvgpr_write_b32 a60, 0\n"
      "v_accvgpr_write_b32 a61, 0\n"
      "v_accvgpr_write_b32 a62, 0\n"
      "v_accvgpr_write_b32 a63, 0\n"
      "global_load_lds_dwordx4 v239, s[42:43]\n"
      "s_add_u32 m0, m0, 1024\n"
      "s_add_u32 s42, s42, 2048\n"
      "s_addc_u32 s43, s43, 0\n"
      "v_accvgpr_write_b32 a64, 0\n"
      "v_accvgpr_write_b32 a65, 0\n"
      "v_accvgpr_write_b32 a66, 0\n"
      "v_accvgpr_write_b32 a67, 0\n"
      "v_accvgpr_write_b32 a68, 0\n"
      "v_accvgpr_write_b32 a69, 0\n"
      "v_accvgpr_write_b32 a70, 0\n"
      "v_accvgpr_write_b32 a71, 0\n"
      "global_load_lds_dwordx4 v239, s[42:43]\n"
      "s_add_u32 m0, m0, 1024\n"
      "s_add_u32 s42, s42, 2048\n"
      "s_addc_u32 s43, s43, 0\n"
      "v_accvgpr_write_b32 a72, 0\n"
      "v_accvgpr_write_b32 a73, 0\n"
      "v_accvgpr_write_b32 a74, 0\n"
      "v_accvgpr_write_b32 a75, 0\n"
      "v_accvgpr_write_b32 a76, 0\n"
      "v_accvgpr_write_b32 a77, 0\n"
      "v_accvgpr_write_b32 a78, 0\n"
      "v_accvgpr_write_b32 a79, 0\n"
      "global_load_lds_dwordx4 v239, s[42:43]\n"
      "s_add_u32 m0, m0, 1024\n"
      "s_add_u32 s42, s42, 2048\n"
      "s_addc_u32 s43, s43, 0\n"
      "v_accvgpr_write_b32 a80, 0\n"
      "v_accvgpr_write_b32 a81, 0\n"
      "v_accvgpr_write_b32 a82, 0\n"
      "v_accvgpr_write_b32 a83, 0\n"
      "v_accvgpr_write_b32 a84, 0\n"
      "v_accvgpr_write_b32 a85, 0\n"
      "v_accvgpr_write_b32 a86, 0\n"
      "v_accvgpr_write_b32 a87, 0\n"
      "global_load_lds_dwordx4 v239, s[42:43]\n"
      "s_add_u32 m0, m0, 1024\n"
      "s_add_u32 s42, s42, 2048\n"
      "s_addc_u32 s43, s43, 0\n"
      "v_accvgpr_write_b32 a88, 0\n"
      "v_accvgpr_write_b32 a89, 0\n"
      "v_accvgpr_write_b32 a90, 0\n"
      "v_accvgpr_write_b32 a91, 0\n"
      "v_accvgpr_write_b32 a92, 0\n"
      "v_accvgpr_write_b32 a93, 0\n"
      "v_accvgpr_write_b32 a94, 0\n"
      "v_accvgpr_write_b32 a95, 0\n"
      "global_load_lds_dwordx4 v239, s[42:43]\n"
      "s_add_u32 m0, m0, 1024\n"
      "s_add_u32 s42, s42, 2048\n"
      "s_addc_u32 s43, s43, 0\n"
      "v_accvgpr_write_b32 a96, 0\n"
      "v_accvgpr_write_b32 a97, 0\n"
      "v_accvgpr_write_b32 a98, 0\n"
      "v_accvgpr_write_b32 a99, 0\n"
      "v_accvgpr_write_b32 a100, 0\n"
      "v_accvgpr_write_b32 a101, 0\n"
      "v_accvgpr_write_b32 a102, 0\n"
      "v_accvgpr_write_b32 a103, 0\n"
      "global_load_lds_dwordx4 v239, s[42:43]\n"
      "s_add_u32 m0, m0, 1024\n"
      "s_add_u32 s42, s42, 2048\n"
      "s_addc_u32 s43, s43, 0\n"
      "v_accvgpr_write_b32 a104, 0\n"
      "v_accvgpr_write_b32 a105, 0\n"
      "v_accvgpr_write_b32 a106, 0\n"
      "v_accvgpr_write_b32 a107, 0\n"
      "v_accvgpr_write_b32 a108, 0\n"
      "v_accvgpr_write_b32 a109, 0\n"
      "v_accvgpr_write_b32 a110, 0\n"
      "v_accvgpr_write_b32 a111, 0\n"
      "global_load_lds_dwordx4 v239, s[42:43]\n"
      "s_add_u32 m0, m0, 1024\n"
      "s_add_u32 s42, s42, 2048\n"
      "s_addc_u32 s43, s43, 0\n"
      "v_accvgpr_write_b32 a112, 0\n"
      "v_accvgpr_write_b32 a113, 0\n"
      "v_accvgpr_write_b32 a114, 0\n"
      "v_accvgpr_write_b32 a115, 0\n"
      "v_accvgpr_write_b32 a116, 0\n"
      "v_accvgpr_write_b32 a117, 0\n"
      "v_accvgpr_write_b32 a118, 0\n"
      "v_accvgpr_write_b32 a119, 0\n"
      "global_load_lds_dwordx4 v239, s[42:43]\n"
      "s_add_u32 m0, m0, 1024\n"
      "s_add_u32 s42, s42, 2048\n"
      "s_addc_u32 s43, s43, 0\n"
      "v_accvgpr_write_b32 a120, 0\n"
      "v_accvgpr_write_b32 a121, 0\n"
      "v_accvgpr_write_b32 a122, 0\n"
      "v_accvgpr_write_b32 a123, 0\n"
      "v_accvgpr_write_b32 a124, 0\n"
      "v_accvgpr_write_b32 a125, 0\n"
      "v_accvgpr_write_b32 a126, 0\n"
      "v_accvgpr_write_b32 a127, 0\n"
      "s_waitcnt vmcnt(31)\n"
      "s_barrier\n"
      "s_mul_i32 s52, %[w], 24\n"
      "s_add_u32 s52, s52, 128000\n"
      "s_and_b32 s61, %[w], 1\n"
      "s_lshl_b32 s61, s61, 2\n"
      "s_add_u32 s61, s61, 128096\n"
      "s_lshl_b32 s53, %[w], 1\n"
      "s_add_u32 s49, s53, 0\n"
      "s_and_b32 s49, s49, 7\n"
      "s_mul_i32 s49, s49, 1664\n"
      "v_add_u32 v92, s49, v91\n"
      "v_add_u32 v93, s52, v92\n"
      "ds_read2_b64 v[0:3], v93 offset0:0 offset1:1\n"
      "ds_read_b64 v[64:65], v93 offset:16\n"
      "v_add_u32 v92, s61, v92\n"
      "ds_read_b32 v80, v92\n"
      "s_add_u32 s49, s53, 1\n"
      "s_and_b32 s49, s49, 7\n"
      "s_mul_i32 s49, s49, 1664\n"
      "v_add_u32 v92, s49, v91\n"
      "v_add_u32 v93, s52, v92\n"
      "ds_read2_b64 v[4:7], v93 offset0:0 offset1:1\n"
      "ds_read_b64 v[66:67], v93 offset:16\n"
      "v_add_u32 v92, s61, v92\n"
      "ds_read_b32 v81, v92\n"
      "s_add_u32 s49, s53, 2\n"
      "s_and_b32 s49, s49, 7\n"
      "s_mul_i32 s49, s49, 1664\n"
      "v_add_u32 v92, s49, v91\n"
      "v_add_u32 v93, s52, v92\n"
      "ds_read2_b64 v[8:11], v93 offset0:0 offset1:1\n"
      "ds_read_b64 v[68:69], v93 offset:16\n"
      "v_add_u32 v92, s61, v92\n"
      "ds_read_b32 v82, v92\n"
      "s_add_u32 s49, s53, 3\n"
      "s_and_b32 s49, s49, 7\n"
      "s_mul_i32 s49, s49, 1664\n"
      "v_add_u32 v92, s49, v91\n"
      "v_add_u32 v93, s52, v92\n"
      "ds_read2_b64 v[12:15], v93 offset0:0 offset1:1\n"
      "ds_read_b64 v[70:71], v93 offset:16\n"
      "v_add_u32 v92, s61, v92\n"
      "ds_read_b32 v83, v92\n"
      "s_add_u32 s49, s53, 4\n"
      "s_and_b32 s49, s49, 7\n"
      "s_mul_i32 s49, s49, 1664\n"
      "v_add_u32 v92, s49, v91\n"
      "v_add_u32 v93, s52, v92\n"
      "ds_read2_b64 v[16:19], v93 offset0:0 offset1:1\n"
      "ds_read_b64 v[72:73], v93 offset:16\n"
      "s_add_u32 s49, s53, 5\n"
      "s_and_b32 s49, s49, 7\n"
      "s_mul_i32 s49, s49, 1664\n"
      "v_add_u32 v92, s49, v91\n"
      "v_add_u32 v93, s52, v92\n"
      "ds_read2_b64 v[20:23], v93 offset0:0 offset1:1\n"
      "ds_read_b64 v[74:75], v93 offset:16\n"
      "s_add_u32 s49, s53, 6\n"
      "s_and_b32 s49, s49, 7\n"
      "s_mul_i32 s49, s49, 1664\n"
      "v_add_u32 v92, s49, v91\n"
      "v_add_u32 v93, s52, v92\n"
      "ds_read2_b64 v[24:27], v93 offset0:0 offset1:1\n"
      "ds_read_b64 v[76:77], v93 offset:16\n"
      "s_add_u32 s49, s53, 7\n"
      "s_and_b32 s49, s49, 7\n"
      "s_mul_i32 s49, s49, 1664\n"
      "v_add_u32 v92, s49, v91\n"
      "v_add_u32 v93, s52, v92\n"
      "ds_read2_b64 v[28:31], v93 offset0:0 offset1:1\n"
      "ds_read_b64 v[78:79], v93 offset:16\n"
      "global_load_lds_dwordx4 v239, s[42:43]\n"
      "s_add_u32 m0, m0, 1024\n"
      "s_add_u32 s42, s42, 2048\n"
      "s_addc_u32 s43, s43, 0\n"
      "global_load_lds_dwordx4 v239, s[42:43]\n"
      "s_add_u32 m0, m0, 1024\n"
      "s_add_u32 s42, s42, 2048\n"
      "s_addc_u32 s43, s43, 0\n"
      "s_waitcnt lgkmcnt(0)\n"
      "v_bitop3_b32 v232, v0, 7, v237 bitop3:0x6a\n"
      "v_lshl_add_u32 v232, v0, 3, v232\n"
      "v_lshlrev_b32 v128, 4, v232\n"
      "v_xor_b32 v180, 64, v128\n"
      "v_bitop3_b32 v232, v1, 7, v237 bitop3:0x6a\n"
      "v_lshl_add_u32 v232, v1, 3, v232\n"
      "v_lshlrev_b32 v136, 4, v232\n"
      "v_xor_b32 v188, 64, v136\n"
      "v_bitop3_b32 v232, v2, 7, v237 bitop3:0x6a\n"
      "v_lshl_add_u32 v232, v2, 3, v232\n"
      "v_lshlrev_b32 v144, 4, v232\n"
      "v_xor_b32 v196, 64, v144\n"
      "v_bitop3_b32 v232, v3, 7, v237 bitop3:0x6a\n"
      "v_lshl_add_u32 v232, v3, 3, v232\n"
      "v_lshlrev_b32 v152, 4, v232\n"
      "v_xor_b32 v204, 64, v152\n"
      "global_load_lds_dwordx4 v239, s[42:43]\n"
      "s_add_u32 m0, m0, 1024\n"
      "s_add_u32 s42, s42, 2048\n"
      "s_addc_u32 s43, s43, 0\n"
      "v_bitop3_b32 v232, v64, 7, v237 bitop3:0x6a\n"
      "v_lshl_add_u32 v232, v64, 3, v232\n"
      "v_lshlrev_b32 v160, 4, v232\n"
      "v_xor_b32 v212, 64, v160\n"
      "v_bitop3_b32 v232, v65, 7, v237 bitop3:0x6a\n"
      "v_lshl_add_u32 v232, v65, 3, v232\n"
      "v_lshlrev_b32 v168, 4, v232\n"
      "v_xor_b32 v220, 64, v168\n"
      "v_bitop3_b32 v232, v80, 7, v237 bitop3:0x6a\n"
      "v_lshl_add_u32 v232, v80, 3, v232\n"
      "v_lshlrev_b32 v176, 4, v232\n"
      "v_xor_b32 v228, 64, v176\n"
      "v_bitop3_b32 v232, v4, 7, v237 bitop3:0x6a\n"
      "v_lshl_add_u32 v232, v4, 3, v232\n"
      "v_lshlrev_b32 v129, 4, v232\n"
      "v_xor_b32 v181, 64, v129\n"
      "global_load_lds_dwordx4 v239, s[42:43]\n"
      "s_add_u32 m0, m0, 1024\n"
      "s_add_u32 s42, s42, 2048\n"
      "s_addc_u32 s43, s43, 0\n"
      "v_bitop3_b32 v232, v5, 7, v237 bitop3:0x6a\n"
      "v_lshl_add_u32 v232, v5, 3, v232\n"
      "v_lshlrev_b32 v137, 4, v232\n"
      "v_xor_b32 v189, 64, v137\n"
      "v_bitop3_b32 v232, v6, 7, v237 bitop3:0x6a\n"
      "v_lshl_add_u32 v232, v6, 3, v232\n"
      "v_lshlrev_b32 v145, 4, v232\n"
      "v_xor_b32 v197, 64, v145\n"
      "v_bitop3_b32 v232, v7, 7, v237 bitop3:0x6a\n"
      "v_lshl_add_u32 v232, v7, 3, v232\n"
      "v_lshlrev_b32 v153, 4, v232\n"
      "v_xor_b32 v205, 64, v153\n"
      "v_bitop3_b32 v232, v66, 7, v237 bitop3:0x6a\n"
      "v_lshl_add_u32 v232, v66, 3, v232\n"
      "v_lshlrev_b32 v161, 4, v232\n"
      "v_xor_b32 v213, 64, v161\n"
      "global_load_lds_dwordx4 v239, s[42:43]\n"
      "s_add_u32 m0, m0, 1024\n"
      "s_add_u32 s42, s42, 2048\n"
      "s_addc_u32 s43, s43, 0\n"
      "v_bitop3_b32 v232, v67, 7, v237 bitop3:0x6a\n"
      "v_lshl_add_u32 v232, v67, 3, v232\n"
      "v_lshlrev_b32 v169, 4, v232\n"
      "v_xor_b32 v221, 64, v169\n"
      "v_bitop3_b32 v232, v81, 7, v237 bitop3:0x6a\n"
      "v_lshl_add_u32 v232, v81, 3, v232\n"
      "v_lshlrev_b32 v177, 4, v232\n"
      "v_xor_b32 v229, 64, v177\n"
      "v_bitop3_b32 v232, v8, 7, v237 bitop3:0x6a\n"
      "v_lshl_add_u32 v232, v8, 3, v232\n"
      "v_lshlrev_b32 v130, 4, v232\n"
      "v_xor_b32 v182, 64, v130\n"
      "v_bitop3_b32 v232, v9, 7, v237 bitop3:0x6a\n"
      "v_lshl_add_u32 v232, v9, 3, v232\n"
      "v_lshlrev_b32 v138, 4, v232\n"
      "v_xor_b32 v190, 64, v138\n"
      "global_load_lds_dwordx4 v239, s[42:43]\n"
      "s_add_u32 m0, m0, 1024\n"
      "s_add_u32 s42, s42, 2048\n"
      "s_addc_u32 s43, s43, 0\n"
      "v_bitop3_b32 v232, v10, 7, v237 bitop3:0x6a\n"
      "v_lshl_add_u32 v232, v10, 3, v232\n"
      "v_lshlrev_b32 v146, 4, v232\n"
      "v_xor_b32 v198, 64, v146\n"
      "v_bitop3_b32 v232, v11, 7, v237 bitop3:0x6a\n"
      "v_lshl_add_u32 v232, v11, 3, v232\n"
      "v_lshlrev_b32 v154, 4, v232\n"
      "v_xor_b32 v206, 64, v154\n"
      "v_bitop3_b32 v232, v68, 7, v237 bitop3:0x6a\n"
      "v_lshl_add_u32 v232, v68, 3, v232\n"
      "v_lshlrev_b32 v162, 4, v232\n"
      "v_xor_b32 v214, 64, v162\n"
      "v_bitop3_b32 v232, v69, 7, v237 bitop3:0x6a\n"
      "v_lshl_add_u32 v232, v69, 3, v232\n"
      "v_lshlrev_b32 v170, 4, v232\n"
      "v_xor_b32 v222, 64, v170\n"
      "global_load_lds_dwordx4 v239, s[42:43]\n"
      "s_add_u32 m0, m0, 1024\n"
      "s_add_u32 s42, s42, 2048\n"
      "s_addc_u32 s43, s43, 0\n"
      "v_bitop3_b32 v232, v82, 7, v237 bitop3:0x6a\n"
      "v_lshl_add_u32 v232, v82, 3, v232\n"
      "v_lshlrev_b32 v178, 4, v232\n"
      "v_xor_b32 v230, 64, v178\n"
      "v_bitop3_b32 v232, v12, 7, v237 bitop3:0x6a\n"
      "v_lshl_add_u32 v232, v12, 3, v232\n"
      "v_lshlrev_b32 v131, 4, v232\n"
      "v_xor_b32 v183, 64, v131\n"
      "v_bitop3_b32 v232, v13, 7, v237 bitop3:0x6a\n"
      "v_lshl_add_u32 v232, v13, 3, v232\n"
      "v_lshlrev_b32 v139, 4, v232\n"
      "v_xor_b32 v191, 64, v139\n"
      "v_bitop3_b32 v232, v14, 7, v237 bitop3:0x6a\n"
      "v_lshl_add_u32 v232, v14, 3, v232\n"
      "v_lshlrev_b32 v147, 4, v232\n"
      "v_xor_b32 v199, 64, v147\n"
      "global_load_lds_dwordx4 v239, s[42:43]\n"
      "s_add_u32 m0, m0, 1024\n"
      "s_add_u32 s42, s42, 2048\n"
      "s_addc_u32 s43, s43, 0\n"
      "v_bitop3_b32 v232, v15, 7, v237 bitop3:0x6a\n"
      "v_lshl_add_u32 v232, v15, 3, v232\n"
      "v_lshlrev_b32 v155, 4, v232\n"
      "v_xor_b32 v207, 64, v155\n"
      "v_bitop3_b32 v232, v70, 7, v237 bitop3:0x6a\n"
      "v_lshl_add_u32 v232, v70, 3, v232\n"
      "v_lshlrev_b32 v163, 4, v232\n"
      "v_xor_b32 v215, 64, v163\n"
      "v_bitop3_b32 v232, v71, 7, v237 bitop3:0x6a\n"
      "v_lshl_add_u32 v232, v71, 3, v232\n"
      "v_lshlrev_b32 v171, 4, v232\n"
      "v_xor_b32 v223, 64, v171\n"
      "v_bitop3_b32 v232, v83, 7, v237 bitop3:0x6a\n"
      "v_lshl_add_u32 v232, v83, 3, v232\n"
      "v_lshlrev_b32 v179, 4, v232\n"
      "v_xor_b32 v231, 64, v179\n"
      "global_load_lds_dwordx4 v239, s[42:43]\n"
      "s_add_u32 m0, m0, 1024\n"
      "s_add_u32 s42, s42, 2048\n"
      "s_addc_u32 s43, s43, 0\n"
      "v_bitop3_b32 v232, v16, 7, v237 bitop3:0x6a\n"
      "v_lshl_add_u32 v232, v16, 3, v232\n"
      "v_lshlrev_b32 v132, 4, v232\n"
      "v_xor_b32 v184, 64, v132\n"
      "v_bitop3_b32 v232, v17, 7, v237 bitop3:0x6a\n"
      "v_lshl_add_u32 v232, v17, 3, v232\n"
      "v_lshlrev_b32 v140, 4, v232\n"
      "v_xor_b32 v192, 64, v140\n"
      "v_bitop3_b32 v232, v18, 7, v237 bitop3:0x6a\n"
      "v_lshl_add_u32 v232, v18, 3, v232\n"
      "v_lshlrev_b32 v148, 4, v232\n"
      "v_xor_b32 v200, 64, v148\n"
      "v_bitop3_b32 v232, v19, 7, v237 bitop3:0x6a\n"
      "v_lshl_add_u32 v232, v19, 3, v232\n"
      "v_lshlrev_b32 v156, 4, v232\n"
      "v_xor_b32 v208, 64, v156\n"
      "global_load_lds_dwordx4 v239, s[42:43]\n"
      "s_add_u32 m0, m0, 1024\n"
      "s_add_u32 s42, s42, 2048\n"
      "s_addc_u32 s43, s43, 0\n"
      "v_bitop3_b32 v232, v72, 7, v237 bitop3:0x6a\n"
      "v_lshl_add_u32 v232, v72, 3, v232\n"
      "v_lshlrev_b32 v164, 4, v232\n"
      "v_xor_b32 v216, 64, v164\n"
      "v_bitop3_b32 v232, v73, 7, v237 bitop3:0x6a\n"
      "v_lshl_add_u32 v232, v73, 3, v232\n"
      "v_lshlrev_b32 v172, 4, v232\n"
      "v_xor_b32 v224, 64, v172\n"
      "v_bitop3_b32 v232, v20, 7, v237 bitop3:0x6a\n"
      "v_lshl_add_u32 v232, v20, 3, v232\n"
      "v_lshlrev_b32 v133, 4, v232\n"
      "v_xor_b32 v185, 64, v133\n"
      "v_bitop3_b32 v232, v21, 7, v237 bitop3:0x6a\n"
      "v_lshl_add_u32 v232, v21, 3, v232\n"
      "v_lshlrev_b32 v141, 4, v232\n"
      "v_xor_b32 v193, 64, v141\n"
      "global_load_lds_dwordx4 v239, s[42:43]\n"
      "s_add_u32 m0, m0, 1024\n"
      "s_add_u32 s42, s42, 2048\n"
      "s_addc_u32 s43, s43, 0\n"
      "v_bitop3_b32 v232, v22, 7, v237 bitop3:0x6a\n"
      "v_lshl_add_u32 v232, v22, 3, v232\n"
      "v_lshlrev_b32 v149, 4, v232\n"
      "v_xor_b32 v201, 64, v149\n"
      "v_bitop3_b32 v232, v23, 7, v237 bitop3:0x6a\n"
      "v_lshl_add_u32 v232, v23, 3, v232\n"
      "v_lshlrev_b32 v157, 4, v232\n"
      "v_xor_b32 v209, 64, v157\n"
      "v_bitop3_b32 v232, v74, 7, v237 bitop3:0x6a\n"
      "v_lshl_add_u32 v232, v74, 3, v232\n"
      "v_lshlrev_b32 v165, 4, v232\n"
      "v_xor_b32 v217, 64, v165\n"
      "v_bitop3_b32 v232, v75, 7, v237 bitop3:0x6a\n"
      "v_lshl_add_u32 v232, v75, 3, v232\n"
      "v_lshlrev_b32 v173, 4, v232\n"
      "v_xor_b32 v225, 64, v173\n"
      "global_load_lds_dwordx4 v239, s[42:43]\n"
      "s_add_u32 m0, m0, 1024\n"
      "s_add_u32 s42, s42, 2048\n"
      "s_addc_u32 s43, s43, 0\n"
      "v_bitop3_b32 v232, v24, 7, v237 bitop3:0x6a\n"
      "v_lshl_add_u32 v232, v24, 3, v232\n"
      "v_lshlrev_b32 v134, 4, v232\n"
      "v_xor_b32 v186, 64, v134\n"
      "v_bitop3_b32 v232, v25, 7, v237 bitop3:0x6a\n"
      "v_lshl_add_u32 v232, v25, 3, v232\n"
      "v_lshlrev_b32 v142, 4, v232\n"
      "v_xor_b32 v194, 64, v142\n"
      "v_bitop3_b32 v232, v26, 7, v237 bitop3:0x6a\n"
      "v_lshl_add_u32 v232, v26, 3, v232\n"
      "v_lshlrev_b32 v150, 4, v232\n"
      "v_xor_b32 v202, 64, v150\n"
      "v_bitop3_b32 v232, v27, 7, v237 bitop3:0x6a\n"
      "v_lshl_add_u32 v232, v27, 3, v232\n"
      "v_lshlrev_b32 v158, 4, v232\n"
      "v_xor_b32 v210, 64, v158\n"
      "global_load_lds_dwordx4 v239, s[42:43]\n"
      "s_add_u32 m0, m0, 1024\n"
      "s_add_u32 s42, s42, 2048\n"
      "s_addc_u32 s43, s43, 0\n"
      "v_bitop3_b32 v232, v76, 7, v237 bitop3:0x6a\n"
      "v_lshl_add_u32 v232, v76, 3, v232\n"
      "v_lshlrev_b32 v166, 4, v232\n"
      "v_xor_b32 v218, 64, v166\n"
      "v_bitop3_b32 v232, v77, 7, v237 bitop3:0x6a\n"
      "v_lshl_add_u32 v232, v77, 3, v232\n"
      "v_lshlrev_b32 v174, 4, v232\n"
      "v_xor_b32 v226, 64, v174\n"
      "v_bitop3_b32 v232, v28, 7, v237 bitop3:0x6a\n"
      "v_lshl_add_u32 v232, v28, 3, v232\n"
      "v_lshlrev_b32 v135, 4, v232\n"
      "v_xor_b32 v187, 64, v135\n"
      "v_bitop3_b32 v232, v29, 7, v237 bitop3:0x6a\n"
      "v_lshl_add_u32 v232, v29, 3, v232\n"
      "v_lshlrev_b32 v143, 4, v232\n"
      "v_xor_b32 v195, 64, v143\n"
      "global_load_lds_dwordx4 v239, s[42:43]\n"
      "s_add_u32 m0, m0, 1024\n"
      "s_add_u32 s42, s42, 2048\n"
      "s_addc_u32 s43, s43, 0\n"
      "v_bitop3_b32 v232, v30, 7, v237 bitop3:0x6a\n"
      "v_lshl_add_u32 v232, v30, 3, v232\n"
      "v_lshlrev_b32 v151, 4, v232\n"
      "v_xor_b32 v203, 64, v151\n"
      "v_bitop3_b32 v232, v31, 7, v237 bitop3:0x6a\n"
      "v_lshl_add_u32 v232, v31, 3, v232\n"
      "v_lshlrev_b32 v159, 4, v232\n"
      "v_xor_b32 v211, 64, v159\n"
      "v_bitop3_b32 v232, v78, 7, v237 bitop3:0x6a\n"
      "v_lshl_add_u32 v232, v78, 3, v232\n"
      "v_lshlrev_b32 v167, 4, v232\n"
      "v_xor_b32 v219, 64, v167\n"
      "v_bitop3_b32 v232, v79, 7, v237 bitop3:0x6a\n"
      "v_lshl_add_u32 v232, v79, 3, v232\n"
      "v_lshlrev_b32 v175, 4, v232\n"
      "v_xor_b32 v227, 64, v175\n"
      "global_load_lds_dwordx4 v239, s[42:43]\n"
      "s_add_u32 m0, m0, 1024\n"
      "s_add_u32 s42, s42, 2048\n"
      "s_addc_u32 s43, s43, 0\n"
      "s_waitcnt vmcnt(42)\n"
      "v_perm_b32 v64, v36, v32, v235\n"
      "v_perm_b32 v232, v36, v32, v236\n"
      "v_perm_b32 v68, v37, v33, v235\n"
      "v_perm_b32 v233, v37, v33, v236\n"
      "v_cndmask_b32_dpp v80, v232, v64, vcc row_ror:8 row_mask:0xf bank_mask:0xf\n"
      "v_mov_b32_dpp v64, v232 row_ror:8 row_mask:0xf bank_mask:0xc\n"
      "v_perm_b32 v72, v38, v34, v235\n"
      "v_perm_b32 v232, v38, v34, v236\n"
      "v_cndmask_b32_dpp v84, v233, v68, vcc row_ror:8 row_mask:0xf bank_mask:0xf\n"
      "v_mov_b32_dpp v68, v233 row_ror:8 row_mask:0xf bank_mask:0xc\n"
      "v_perm_b32 v76, v39, v35, v235\n"
      "v_perm_b32 v233, v39, v35, v236\n"
      "v_cndmask_b32_dpp v88, v232, v72, vcc row_ror:8 row_mask:0xf bank_mask:0xf\n"
      "v_mov_b32_dpp v72, v232 row_ror:8 row_mask:0xf bank_mask:0xc\n"
      "s_waitcnt vmcnt(40)\n"
      "v_perm_b32 v65, v44, v40, v235\n"
      "v_perm_b32 v232, v44, v40, v236\n"
      "v_cndmask_b32_dpp v92, v233, v76, vcc row_ror:8 row_mask:0xf bank_mask:0xf\n"
      "v_mov_b32_dpp v76, v233 row_ror:8 row_mask:0xf bank_mask:0xc\n"
      "v_perm_b32 v69, v45, v41, v235\n"
      "v_perm_b32 v233, v45, v41, v236\n"
      "v_cndmask_b32_dpp v81, v232, v65, vcc row_ror:8 row_mask:0xf bank_mask:0xf\n"
      "v_mov_b32_dpp v65, v232 row_ror:8 row_mask:0xf bank_mask:0xc\n"
      "v_perm_b32 v73, v46, v42, v235\n"
      "v_perm_b32 v232, v46, v42, v236\n"
      "v_cndmask_b32_dpp v85, v233, v69, vcc row_ror:8 row_mask:0xf bank_mask:0xf\n"
      "v_mov_b32_dpp v69, v233 row_ror:8 row_mask:0xf bank_mask:0xc\n"
      "v_perm_b32 v77, v47, v43, v235\n"
      "v_perm_b32 v233, v47, v43, v236\n"
      "v_cndmask_b32_dpp v89, v232, v73, vcc row_ror:8 row_mask:0xf bank_mask:0xf\n"
      "v_mov_b32_dpp v73, v232 row_ror:8 row_mask:0xf bank_mask:0xc\n"
      "s_waitcnt vmcnt(38)\n"
      "v_perm_b32 v66, v52, v48, v235\n"
      "v_perm_b32 v232, v52, v48, v236\n"
      "v_cndmask_b32_dpp v93, v233, v77, vcc row_ror:8 row_mask:0xf bank_mask:0xf\n"
      "v_mov_b32_dpp v77, v233 row_ror:8 row_mask:0xf bank_mask:0xc\n"
      "v_perm_b32 v70, v53, v49, v235\n"
      "v_perm_b32 v233, v53, v49, v236\n"
      "v_cndmask_b32_dpp v82, v232, v66, vcc row_ror:8 row_mask:0xf bank_mask:0xf\n"
      "v_mov_b32_dpp v66, v232 row_ror:8 row_mask:0xf bank_mask:0xc\n"
      "v_perm_b32 v74, v54, v50, v235\n"
      "v_perm_b32 v232, v54, v50, v236\n"
      "v_cndmask_b32_dpp v86, v233, v70, vcc row_ror:8 row_mask:0xf bank_mask:0xf\n"
      "v_mov_b32_dpp v70, v233 row_ror:8 row_mask:0xf bank_mask:0xc\n"
      "v_perm_b32 v78, v55, v51, v235\n"
      "v_perm_b32 v233, v55, v51, v236\n"
      "v_cndmask_b32_dpp v90, v232, v74, vcc row_ror:8 row_mask:0xf bank_mask:0xf\n"
      "v_mov_b32_dpp v74, v232 row_ror:8 row_mask:0xf bank_mask:0xc\n"
      "s_waitcnt vmcnt(36)\n"
      "v_perm_b32 v67, v60, v56, v235\n"
      "v_perm_b32 v232, v60, v56, v236\n"
      "v_cndmask_b32_dpp v94, v233, v78, vcc row_ror:8 row_mask:0xf bank_mask:0xf\n"
      "v_mov_b32_dpp v78, v233 row_ror:8 row_mask:0xf bank_mask:0xc\n"
      "v_perm_b32 v71, v61, v57, v235\n"
      "v_perm_b32 v233, v61, v57, v236\n"
      "v_cndmask_b32_dpp v83, v232, v67, vcc row_ror:8 row_mask:0xf bank_mask:0xf\n"
      "v_mov_b32_dpp v67, v232 row_ror:8 row_mask:0xf bank_mask:0xc\n"
      "v_perm_b32 v75, v62, v58, v235\n"
      "v_perm_b32 v232, v62, v58, v236\n"
      "v_cndmask_b32_dpp v87, v233, v71, vcc row_ror:8 row_mask:0xf bank_mask:0xf\n"
      "v_mov_b32_dpp v71, v233 row_ror:8 row_mask:0xf bank_mask:0xc\n"
      "v_perm_b32 v79, v63, v59, v235\n"
      "v_perm_b32 v233, v63, v59, v236\n"
      "v_cndmask_b32_dpp v91, v232, v75, vcc row_ror:8 row_mask:0xf bank_mask:0xf\n"
      "v_mov_b32_dpp v75, v232 row_ror:8 row_mask:0xf bank_mask:0xc\n"
      "s_nop 1\n"
      "v_cndmask_b32_dpp v95, v233, v79, vcc row_ror:8 row_mask:0xf bank_mask:0xf\n"
      "v_mov_b32_dpp v79, v233 row_ror:8 row_mask:0xf bank_mask:0xc\n"
      "s_mov_b64 exec, 0xffff\n"
      "global_load_lds_dwordx4 v239, s[42:43]\n"
      "s_mov_b64 exec, -1\n"
      "s_add_u32 s40, s40, 131072\n"
      "s_addc_u32 s41, s41, 0\n"
      "global_load_dwordx4 v[32:35], v234, s[40:41] offset:-4096\n"
      "global_load_dwordx4 v[36:39], v234, s[40:41] offset:-3072\n"
      "global_load_dwordx4 v[40:43], v234, s[40:41] offset:-2048\n"
      "global_load_dwordx4 v[44:47], v234, s[40:41] offset:-1024\n"
      "global_load_dwordx4 v[48:51], v234, s[40:41] offset:0\n"
      "global_load_dwordx4 v[52:55], v234, s[40:41] offset:1024\n"
      "global_load_dwordx4 v[56:59], v234, s[40:41] offset:2048\n"
      "global_load_dwordx4 v[60:63], v234, s[40:41] offset:3072\n"
      "s_waitcnt vmcnt(8)\n"
      "2:\n"
      "s_barrier\n"
      "ds_read_b128 a[128:131], v128\n"
      "ds_read_b128 a[132:135], v129\n"
      "ds_read_b128 a[136:139], v130\n"
      "ds_read_b128 a[140:143], v131\n"
      "ds_read_b128 a[144:147], v132\n"
      "ds_read_b128 a[148:151], v133\n"
      "ds_read_b128 a[152:155], v134\n"
      "ds_read_b128 a[156:159], v135\n"
      "s_add_u32 s40, s40, 131072\n"
      "s_addc_u32 s41, s41, 0\n"
      "s_waitcnt lgkmcnt(7)\n"
      "v_mfma_f32_16x16x32_bf16 a[0:3], v[64:67], a[128:131], a[0:3]\n"
      "s_waitcnt vmcnt(6)\n"
      "v_perm_b32 v96, v36, v32, v235\n"
      "v_perm_b32 v232, v36, v32, v236\n"
      "v_mfma_f32_16x16x32_bf16 a[4:7], v[68:71], a[128:131], a[4:7]\n"
      "ds_read_b128 a[160:163], v180\n"
      "v_mfma_f32_16x16x32_bf16 a[8:11], v[72:75], a[128:131], a[8:11]\n"
      "global_load_dwordx4 v[0:3], v234, s[40:41] offset:-4096\n"
      "v_mfma_f32_16x16x32_bf16 a[12:15], v[76:79], a[128:131], a[12:15]\n"
      "v_cndmask_b32_dpp v112, v232, v96, vcc row_ror:8 row_mask:0xf bank_mask:0xf\n"
      "v_mov_b32_dpp v96, v232 row_ror:8 row_mask:0xf bank_mask:0xc\n"
      "s_waitcnt lgkmcnt(7)\n"
      "v_mfma_f32_16x16x32_bf16 a[16:19], v[64:67], a[132:135], a[16:19]\n"
      "v_perm_b32 v100, v37, v33, v235\n"
      "v_perm_b32 v233, v37, v33, v236\n"
      "v_mfma_f32_16x16x32_bf16 a[20:23], v[68:71], a[132:135], a[20:23]\n"
      "ds_read_b128 a[164:167], v181\n"
      "v_mfma_f32_16x16x32_bf16 a[24:27], v[72:75], a[132:135], a[24:27]\n"
      "global_load_dwordx4 v[4:7], v234, s[40:41] offset:-3072\n"
      "v_mfma_f32_16x16x32_bf16 a[28:31], v[76:79], a[132:135], a[28:31]\n"
      "v_cndmask_b32_dpp v116, v233, v100, vcc row_ror:8 row_mask:0xf bank_mask:0xf\n"
      "v_mov_b32_dpp v100, v233 row_ror:8 row_mask:0xf bank_mask:0xc\n"
      "s_waitcnt lgkmcnt(7)\n"
      "v_mfma_f32_16x16x32_bf16 a[32:35], v[64:67], a[136:139], a[32:35]\n"
      "v_perm_b32 v104, v38, v34, v235\n"
      "v_perm_b32 v232, v38, v34, v236\n"
      "v_mfma_f32_16x16x32_bf16 a[36:39], v[68:71], a[136:139], a[36:39]\n"
      "ds_read_b128 a[168:171], v182\n"
      "v_mfma_f32_16x16x32_bf16 a[40:43], v[72:75], a[136:139], a[40:43]\n"
      "global_load_dwordx4 v[8:11], v234, s[40:41] offset:-2048\n"
      "v_mfma_f32_16x16x32_bf16 a[44:47], v[76:79], a[136:139], a[44:47]\n"
      "v_cndmask_b32_dpp v120, v232, v104, vcc row_ror:8 row_mask:0xf bank_mask:0xf\n"
      "v_mov_b32_dpp v104, v232 row_ror:8 row_mask:0xf bank_mask:0xc\n"
      "s_waitcnt lgkmcnt(7)\n"
      "v_mfma_f32_16x16x32_bf16 a[48:51], v[64:67], a[140:143], a[48:51]\n"
      "v_perm_b32 v108, v39, v35, v235\n"
      "v_perm_b32 v233, v39, v35, v236\n"
      "v_mfma_f32_16x16x32_bf16 a[52:55], v[68:71], a[140:143], a[52:55]\n"
      "ds_read_b128 a[172:175], v183\n"
      "v_mfma_f32_16x16x32_bf16 a[56:59], v[72:75], a[140:143], a[56:59]\n"
      "global_load_dwordx4 v[12:15], v234, s[40:41] offset:-1024\n"
      "v_mfma_f32_16x16x32_bf16 a[60:63], v[76:79], a[140:143], a[60:63]\n"
      "v_cndmask_b32_dpp v124, v233, v108, vcc row_ror:8 row_mask:0xf bank_mask:0xf\n"
      "v_mov_b32_dpp v108, v233 row_ror:8 row_mask:0xf bank_mask:0xc\n"
      "s_waitcnt lgkmcnt(7)\n"
      "v_mfma_f32_16x16x32_bf16 a[64:67], v[64:67], a[144:147], a[64:67]\n"
      "s_waitcnt vmcnt(8)\n"
      "v_perm_b32 v97, v44, v40, v235\n"
      "v_perm_b32 v232, v44, v40, v236\n"
      "v_mfma_f32_16x16x32_bf16 a[68:71], v[68:71], a[144:147], a[68:71]\n"
      "ds_read_b128 a[176:179], v184\n"
      "v_mfma_f32_16x16x32_bf16 a[72:75], v[72:75], a[144:147], a[72:75]\n"
      "global_load_dwordx4 v[16:19], v234, s[40:41] offset:0\n"
      "v_mfma_f32_16x16x32_bf16 a[76:79], v[76:79], a[144:147], a[76:79]\n"
      "v_cndmask_b32_dpp v113, v232, v97, vcc row_ror:8 row_mask:0xf bank_mask:0xf\n"
      "v_mov_b32_dpp v97, v232 row_ror:8 row_mask:0xf bank_mask:0xc\n"
      "s_waitcnt lgkmcnt(7)\n"
      "v_mfma_f32_16x16x32_bf16 a[80:83], v[64:67], a[148:151], a[80:83]\n"
      "v_perm_b32 v101, v45, v41, v235\n"
      "v_perm_b32 v233, v45, v41, v236\n"
      "v_mfma_f32_16x16x32_bf16 a[84:87], v[68:71], a[148:151], a[84:87]\n"
      "ds_read_b128 a[180:183], v185\n"
      "v_mfma_f32_16x16x32_bf16 a[88:91], v[72:75], a[148:151], a[88:91]\n"
      "global_load_dwordx4 v[20:23], v234, s[40:41] offset:1024\n"
      "v_mfma_f32_16x16x32_bf16 a[92:95], v[76:79], a[148:151], a[92:95]\n"
      "v_cndmask_b32_dpp v117, v233, v101, vcc row_ror:8 row_mask:0xf bank_mask:0xf\n"
      "v_mov_b32_dpp v101, v233 row_ror:8 row_mask:0xf bank_mask:0xc\n"
      "s_waitcnt lgkmcnt(7)\n"
      "v_mfma_f32_16x16x32_bf16 a[96:99], v[64:67], a[152:155], a[96:99]\n"
      "v_perm_b32 v105, v46, v42, v235\n"
      "v_perm_b32 v232, v46, v42, v236\n"
      "v_mfma_f32_16x16x32_bf16 a[100:103], v[68:71], a[152:155], a[100:103]\n"
      "ds_read_b128 a[184:187], v186\n"
      "v_mfma_f32_16x16x32_bf16 a[104:107], v[72:75], a[152:155], a[104:107]\n"
      "global_load_dwordx4 v[24:27], v234, s[40:41] offset:2048\n"
      "v_mfma_f32_16x16x32_bf16 a[108:111], v[76:79], a[152:155], a[108:111]\n"
      "v_cndmask_b32_dpp v121, v232, v105, vcc row_ror:8 row_mask:0xf bank_mask:0xf\n"
      "v_mov_b32_dpp v105, v232 row_ror:8 row_mask:0xf bank_mask:0xc\n"
      "s_waitcnt lgkmcnt(7)\n"
      "v_mfma_f32_16x16x32_bf16 a[112:115], v[64:67], a[156:159], a[112:115]\n"
      "v_perm_b32 v109, v47, v43, v235\n"
      "v_perm_b32 v233, v47, v43, v236\n"
      "v_mfma_f32_16x16x32_bf16 a[116:119], v[68:71], a[156:159], a[116:119]\n"
      "ds_read_b128 a[188:191], v187\n"
      "v_mfma_f32_16x16x32_bf16 a[120:123], v[72:75], a[156:159], a[120:123]\n"
      "global_load_dwordx4 v[28:31], v234, s[40:41] offset:3072\n"
      "v_mfma_f32_16x16x32_bf16 a[124:127], v[76:79], a[156:159], a[124:127]\n"
      "v_cndmask_b32_dpp v125, v233, v109, vcc row_ror:8 row_mask:0xf bank_mask:0xf\n"
      "v_mov_b32_dpp v109, v233 row_ror:8 row_mask:0xf bank_mask:0xc\n"
      "s_waitcnt lgkmcnt(7)\n"
      "v_mfma_f32_16x16x32_bf16 a[0:3], v[80:83], a[160:163], a[0:3]\n"
      "s_waitcnt vmcnt(10)\n"
      "v_perm_b32 v98, v52, v48, v235\n"
      "v_perm_b32 v232, v52, v48, v236\n"
      "v_mfma_f32_16x16x32_bf16 a[4:7], v[84:87], a[160:163], a[4:7]\n"
      "ds_read_b128 a[128:131], v136\n"
      "v_mfma_f32_16x16x32_bf16 a[8:11], v[88:91], a[160:163], a[8:11]\n"
      "v_mfma_f32_16x16x32_bf16 a[12:15], v[92:95], a[160:163], a[12:15]\n"
      "v_cndmask_b32_dpp v114, v232, v98, vcc row_ror:8 row_mask:0xf bank_mask:0xf\n"
      "v_mov_b32_dpp v98, v232 row_ror:8 row_mask:0xf bank_mask:0xc\n"
      "s_waitcnt lgkmcnt(7)\n"
      "v_mfma_f32_16x16x32_bf16 a[16:19], v[80:83], a[164:167], a[16:19]\n"
      "v_perm_b32 v102, v53, v49, v235\n"
      "v_perm_b32 v233, v53, v49, v236\n"
      "v_mfma_f32_16x16x32_bf16 a[20:23], v[84:87], a[164:167], a[20:23]\n"
      "ds_read_b128 a[132:135], v137\n"
      "v_mfma_f32_16x16x32_bf16 a[24:27], v[88:91], a[164:167], a[24:27]\n"
      "v_mfma_f32_16x16x32_bf16 a[28:31], v[92:95], a[164:167], a[28:31]\n"
      "v_cndmask_b32_dpp v118, v233, v102, vcc row_ror:8 row_mask:0xf bank_mask:0xf\n"
      "v_mov_b32_dpp v102, v233 row_ror:8 row_mask:0xf bank_mask:0xc\n"
      "s_waitcnt lgkmcnt(7)\n"
      "v_mfma_f32_16x16x32_bf16 a[32:35], v[80:83], a[168:171], a[32:35]\n"
      "v_perm_b32 v106, v54, v50, v235\n"
      "v_perm_b32 v232, v54, v50, v236\n"
      "v_mfma_f32_16x16x32_bf16 a[36:39], v[84:87], a[168:171], a[36:39]\n"
      "ds_read_b128 a[136:139], v138\n"
      "v_mfma_f32_16x16x32_bf16 a[40:43], v[88:91], a[168:171], a[40:43]\n"
      "v_mfma_f32_16x16x32_bf16 a[44:47], v[92:95], a[168:171], a[44:47]\n"
      "v_cndmask_b32_dpp v122, v232, v106, vcc row_ror:8 row_mask:0xf bank_mask:0xf\n"
      "v_mov_b32_dpp v106, v232 row_ror:8 row_mask:0xf bank_mask:0xc\n"
      "s_waitcnt lgkmcnt(7)\n"
      "v_mfma_f32_16x16x32_bf16 a[48:51], v[80:83], a[172:175], a[48:51]\n"
      "v_perm_b32 v110, v55, v51, v235\n"
      "v_perm_b32 v233, v55, v51, v236\n"
      "v_mfma_f32_16x16x32_bf16 a[52:55], v[84:87], a[172:175], a[52:55]\n"
      "ds_read_b128 a[140:143], v139\n"
      "v_mfma_f32_16x16x32_bf16 a[56:59], v[88:91], a[172:175], a[56:59]\n"
      "v_mfma_f32_16x16x32_bf16 a[60:63], v[92:95], a[172:175], a[60:63]\n"
      "v_cndmask_b32_dpp v126, v233, v110, vcc row_ror:8 row_mask:0xf bank_mask:0xf\n"
      "v_mov_b32_dpp v110, v233 row_ror:8 row_mask:0xf bank_mask:0xc\n"
      "s_waitcnt lgkmcnt(7)\n"
      "v_mfma_f32_16x16x32_bf16 a[64:67], v[80:83], a[176:179], a[64:67]\n"
      "s_waitcnt vmcnt(8)\n"
      "v_perm_b32 v99, v60, v56, v235\n"
      "v_perm_b32 v232, v60, v56, v236\n"
      "v_mfma_f32_16x16x32_bf16 a[68:71], v[84:87], a[176:179], a[68:71]\n"
      "ds_read_b128 a[144:147], v140\n"
      "v_mfma_f32_16x16x32_bf16 a[72:75], v[88:91], a[176:179], a[72:75]\n"
      "v_mfma_f32_16x16x32_bf16 a[76:79], v[92:95], a[176:179], a[76:79]\n"
      "v_cndmask_b32_dpp v115, v232, v99, vcc row_ror:8 row_mask:0xf bank_mask:0xf\n"
      "v_mov_b32_dpp v99, v232 row_ror:8 row_mask:0xf bank_mask:0xc\n"
      "s_waitcnt lgkmcnt(7)\n"
      "v_mfma_f32_16x16x32_bf16 a[80:83], v[80:83], a[180:183], a[80:83]\n"
      "v_perm_b32 v103, v61, v57, v235\n"
      "v_perm_b32 v233, v61, v57, v236\n"
      "v_mfma_f32_16x16x32_bf16 a[84:87], v[84:87], a[180:183], a[84:87]\n"
      "ds_read_b128 a[148:151], v141\n"
      "v_mfma_f32_16x16x32_bf16 a[88:91], v[88:91], a[180:183], a[88:91]\n"
      "v_mfma_f32_16x16x32_bf16 a[92:95], v[92:95], a[180:183], a[92:95]\n"
      "v_cndmask_b32_dpp v119, v233, v103, vcc row_ror:8 row_mask:0xf bank_mask:0xf\n"
      "v_mov_b32_dpp v103, v233 row_ror:8 row_mask:0xf bank_mask:0xc\n"
      "s_waitcnt lgkmcnt(7)\n"
      "v_mfma_f32_16x16x32_bf16 a[96:99], v[80:83], a[184:187], a[96:99]\n"
      "v_perm_b32 v107, v62, v58, v235\n"
      "v_perm_b32 v232, v62, v58, v236\n"
      "v_mfma_f32_16x16x32_bf16 a[100:103], v[84:87], a[184:187], a[100:103]\n"
      "ds_read_b128 a[152:155], v142\n"
      "v_mfma_f32_16x16x32_bf16 a[104:107], v[88:91], a[184:187], a[104:107]\n"
      "v_mfma_f32_16x16x32_bf16 a[108:111], v[92:95], a[184:187], a[108:111]\n"
      "v_cndmask_b32_dpp v123, v232, v107, vcc row_ror:8 row_mask:0xf bank_mask:0xf\n"
      "v_mov_b32_dpp v107, v232 row_ror:8 row_mask:0xf bank_mask:0xc\n"
      "s_waitcnt lgkmcnt(7)\n"
      "v_mfma_f32_16x16x32_bf16 a[112:115], v[80:83], a[188:191], a[112:115]\n"
      "v_perm_b32 v111, v63, v59, v235\n"
      "v_perm_b32 v233, v63, v59, v236\n"
      "v_mfma_f32_16x16x32_bf16 a[116:119], v[84:87], a[188:191], a[116:119]\n"
      "ds_read_b128 a[156:159], v143\n"
      "v_mfma_f32_16x16x32_bf16 a[120:123], v[88:91], a[188:191], a[120:123]\n"
      "v_mfma_f32_16x16x32_bf16 a[124:127], v[92:95], a[188:191], a[124:127]\n"
      "v_cndmask_b32_dpp v127, v233, v111, vcc row_ror:8 row_mask:0xf bank_mask:0xf\n"
      "v_mov_b32_dpp v111, v233 row_ror:8 row_mask:0xf bank_mask:0xc\n"
      "s_add_u32 s40, s40, 131072\n"
      "s_addc_u32 s41, s41, 0\n"
      "s_waitcnt lgkmcnt(7)\n"
      "v_mfma_f32_16x16x32_bf16 a[0:3], v[96:99], a[128:131], a[0:3]\n"
      "s_waitcnt vmcnt(6)\n"
      "v_perm_b32 v64, v4, v0, v235\n"
      "v_perm_b32 v232, v4, v0, v236\n"
      "v_mfma_f32_16x16x32_bf16 a[4:7], v[100:103], a[128:131], a[4:7]\n"
      "ds_read_b128 a[160:163], v188\n"
      "v_mfma_f32_16x16x32_bf16 a[8:11], v[104:107], a[128:131], a[8:11]\n"
      "global_load_dwordx4 v[32:35], v234, s[40:41] offset:-4096\n"
      "v_mfma_f32_16x16x32_bf16 a[12:15], v[108:111], a[128:131], a[12:15]\n"
      "v_cndmask_b32_dpp v80, v232, v64, vcc row_ror:8 row_mask:0xf bank_mask:0xf\n"
      "v_mov_b32_dpp v64, v232 row_ror:8 row_mask:0xf bank_mask:0xc\n"
      "s_waitcnt lgkmcnt(7)\n"
      "v_mfma_f32_16x16x32_bf16 a[16:19], v[96:99], a[132:135], a[16:19]\n"
      "v_perm_b32 v68, v5, v1, v235\n"
      "v_perm_b32 v233, v5, v1, v236\n"
      "v_mfma_f32_16x16x32_bf16 a[20:23], v[100:103], a[132:135], a[20:23]\n"
      "ds_read_b128 a[164:167], v189\n"
      "v_mfma_f32_16x16x32_bf16 a[24:27], v[104:107], a[132:135], a[24:27]\n"
      "global_load_dwordx4 v[36:39], v234, s[40:41] offset:-3072\n"
      "v_mfma_f32_16x16x32_bf16 a[28:31], v[108:111], a[132:135], a[28:31]\n"
      "v_cndmask_b32_dpp v84, v233, v68, vcc row_ror:8 row_mask:0xf bank_mask:0xf\n"
      "v_mov_b32_dpp v68, v233 row_ror:8 row_mask:0xf bank_mask:0xc\n"
      "s_waitcnt lgkmcnt(7)\n"
      "v_mfma_f32_16x16x32_bf16 a[32:35], v[96:99], a[136:139], a[32:35]\n"
      "v_perm_b32 v72, v6, v2, v235\n"
      "v_perm_b32 v232, v6, v2, v236\n"
      "v_mfma_f32_16x16x32_bf16 a[36:39], v[100:103], a[136:139], a[36:39]\n"
      "ds_read_b128 a[168:171], v190\n"
      "v_mfma_f32_16x16x32_bf16 a[40:43], v[104:107], a[136:139], a[40:43]\n"
      "global_load_dwordx4 v[40:43], v234, s[40:41] offset:-2048\n"
      "v_mfma_f32_16x16x32_bf16 a[44:47], v[108:111], a[136:139], a[44:47]\n"
      "v_cndmask_b32_dpp v88, v232, v72, vcc row_ror:8 row_mask:0xf bank_mask:0xf\n"
      "v_mov_b32_dpp v72, v232 row_ror:8 row_mask:0xf bank_mask:0xc\n"
      "s_waitcnt lgkmcnt(7)\n"
      "v_mfma_f32_16x16x32_bf16 a[48:51], v[96:99], a[140:143], a[48:51]\n"
      "v_perm_b32 v76, v7, v3, v235\n"
      "v_perm_b32 v233, v7, v3, v236\n"
      "v_mfma_f32_16x16x32_bf16 a[52:55], v[100:103], a[140:143], a[52:55]\n"
      "ds_read_b128 a[172:175], v191\n"
      "v_mfma_f32_16x16x32_bf16 a[56:59], v[104:107], a[140:143], a[56:59]\n"
      "global_load_dwordx4 v[44:47], v234, s[40:41] offset:-1024\n"
      "v_mfma_f32_16x16x32_bf16 a[60:63], v[108:111], a[140:143], a[60:63]\n"
      "v_cndmask_b32_dpp v92, v233, v76, vcc row_ror:8 row_mask:0xf bank_mask:0xf\n"
      "v_mov_b32_dpp v76, v233 row_ror:8 row_mask:0xf bank_mask:0xc\n"
      "s_waitcnt lgkmcnt(7)\n"
      "v_mfma_f32_16x16x32_bf16 a[64:67], v[96:99], a[144:147], a[64:67]\n"
      "s_waitcnt vmcnt(8)\n"
      "v_perm_b32 v65, v12, v8, v235\n"
      "v_perm_b32 v232, v12, v8, v236\n"
      "v_mfma_f32_16x16x32_bf16 a[68:71], v[100:103], a[144:147], a[68:71]\n"
      "ds_read_b128 a[176:179], v192\n"
      "v_mfma_f32_16x16x32_bf16 a[72:75], v[104:107], a[144:147], a[72:75]\n"
      "global_load_dwordx4 v[48:51], v234, s[40:41] offset:0\n"
      "v_mfma_f32_16x16x32_bf16 a[76:79], v[108:111], a[144:147], a[76:79]\n"
      "v_cndmask_b32_dpp v81, v232, v65, vcc row_ror:8 row_mask:0xf bank_mask:0xf\n"
      "v_mov_b32_dpp v65, v232 row_ror:8 row_mask:0xf bank_mask:0xc\n"
      "s_waitcnt lgkmcnt(7)\n"
      "v_mfma_f32_16x16x32_bf16 a[80:83], v[96:99], a[148:151], a[80:83]\n"
      "v_perm_b32 v69, v13, v9, v235\n"
      "v_perm_b32 v233, v13, v9, v236\n"
      "v_mfma_f32_16x16x32_bf16 a[84:87], v[100:103], a[148:151], a[84:87]\n"
      "ds_read_b128 a[180:183], v193\n"
      "v_mfma_f32_16x16x32_bf16 a[88:91], v[104:107], a[148:151], a[88:91]\n"
      "global_load_dwordx4 v[52:55], v234, s[40:41] offset:1024\n"
      "v_mfma_f32_16x16x32_bf16 a[92:95], v[108:111], a[148:151], a[92:95]\n"
      "v_cndmask_b32_dpp v85, v233, v69, vcc row_ror:8 row_mask:0xf bank_mask:0xf\n"
      "v_mov_b32_dpp v69, v233 row_ror:8 row_mask:0xf bank_mask:0xc\n"
      "s_waitcnt lgkmcnt(7)\n"
      "v_mfma_f32_16x16x32_bf16 a[96:99], v[96:99], a[152:155], a[96:99]\n"
      "v_perm_b32 v73, v14, v10, v235\n"
      "v_perm_b32 v232, v14, v10, v236\n"
      "v_mfma_f32_16x16x32_bf16 a[100:103], v[100:103], a[152:155], a[100:103]\n"
      "ds_read_b128 a[184:187], v194\n"
      "v_mfma_f32_16x16x32_bf16 a[104:107], v[104:107], a[152:155], a[104:107]\n"
      "global_load_dwordx4 v[56:59], v234, s[40:41] offset:2048\n"
      "v_mfma_f32_16x16x32_bf16 a[108:111], v[108:111], a[152:155], a[108:111]\n"
      "v_cndmask_b32_dpp v89, v232, v73, vcc row_ror:8 row_mask:0xf bank_mask:0xf\n"
      "v_mov_b32_dpp v73, v232 row_ror:8 row_mask:0xf bank_mask:0xc\n"
      "s_waitcnt lgkmcnt(7)\n"
      "v_mfma_f32_16x16x32_bf16 a[112:115], v[96:99], a[156:159], a[112:115]\n"
      "v_perm_b32 v77, v15, v11, v235\n"
      "v_perm_b32 v233, v15, v11, v236\n"
      "v_mfma_f32_16x16x32_bf16 a[116:119], v[100:103], a[156:159], a[116:119]\n"
      "ds_read_b128 a[188:191], v195\n"
      "v_mfma_f32_16x16x32_bf16 a[120:123], v[104:107], a[156:159], a[120:123]\n"
      "global_load_dwordx4 v[60:63], v234, s[40:41] offset:3072\n"
      "v_mfma_f32_16x16x32_bf16 a[124:127], v[108:111], a[156:159], a[124:127]\n"
      "v_cndmask_b32_dpp v93, v233, v77, vcc row_ror:8 row_mask:0xf bank_mask:0xf\n"
      "v_mov_b32_dpp v77, v233 row_ror:8 row_mask:0xf bank_mask:0xc\n"
      "s_waitcnt lgkmcnt(7)\n"
      "v_mfma_f32_16x16x32_bf16 a[0:3], v[112:115], a[160:163], a[0:3]\n"
      "s_waitcnt vmcnt(10)\n"
      "v_perm_b32 v66, v20, v16, v235\n"
      "v_perm_b32 v232, v20, v16, v236\n"
      "v_mfma_f32_16x16x32_bf16 a[4:7], v[116:119], a[160:163], a[4:7]\n"
      "ds_read_b128 a[128:131], v144\n"
      "v_mfma_f32_16x16x32_bf16 a[8:11], v[120:123], a[160:163], a[8:11]\n"
      "v_mfma_f32_16x16x32_bf16 a[12:15], v[124:127], a[160:163], a[12:15]\n"
      "v_cndmask_b32_dpp v82, v232, v66, vcc row_ror:8 row_mask:0xf bank_mask:0xf\n"
      "v_mov_b32_dpp v66, v232 row_ror:8 row_mask:0xf bank_mask:0xc\n"
      "s_waitcnt lgkmcnt(7)\n"
      "v_mfma_f32_16x16x32_bf16 a[16:19], v[112:115], a[164:167], a[16:19]\n"
      "v_perm_b32 v70, v21, v17, v235\n"
      "v_perm_b32 v233, v21, v17, v236\n"
      "v_mfma_f32_16x16x32_bf16 a[20:23], v[116:119], a[164:167], a[20:23]\n"
      "ds_read_b128 a[132:135], v145\n"
      "v_mfma_f32_16x16x32_bf16 a[24:27], v[120:123], a[164:167], a[24:27]\n"
      "v_mfma_f32_16x16x32_bf16 a[28:31], v[124:127], a[164:167], a[28:31]\n"
      "v_cndmask_b32_dpp v86, v233, v70, vcc row_ror:8 row_mask:0xf bank_mask:0xf\n"
      "v_mov_b32_dpp v70, v233 row_ror:8 row_mask:0xf bank_mask:0xc\n"
      "s_waitcnt lgkmcnt(7)\n"
      "v_mfma_f32_16x16x32_bf16 a[32:35], v[112:115], a[168:171], a[32:35]\n"
      "v_perm_b32 v74, v22, v18, v235\n"
      "v_perm_b32 v232, v22, v18, v236\n"
      "v_mfma_f32_16x16x32_bf16 a[36:39], v[116:119], a[168:171], a[36:39]\n"
      "ds_read_b128 a[136:139], v146\n"
      "v_mfma_f32_16x16x32_bf16 a[40:43], v[120:123], a[168:171], a[40:43]\n"
      "v_mfma_f32_16x16x32_bf16 a[44:47], v[124:127], a[168:171], a[44:47]\n"
      "v_cndmask_b32_dpp v90, v232, v74, vcc row_ror:8 row_mask:0xf bank_mask:0xf\n"
      "v_mov_b32_dpp v74, v232 row_ror:8 row_mask:0xf bank_mask:0xc\n"
      "s_waitcnt lgkmcnt(7)\n"
      "v_mfma_f32_16x16x32_bf16 a[48:51], v[112:115], a[172:175], a[48:51]\n"
      "v_perm_b32 v78, v23, v19, v235\n"
      "v_perm_b32 v233, v23, v19, v236\n"
      "v_mfma_f32_16x16x32_bf16 a[52:55], v[116:119], a[172:175], a[52:55]\n"
      "ds_read_b128 a[140:143], v147\n"
      "v_mfma_f32_16x16x32_bf16 a[56:59], v[120:123], a[172:175], a[56:59]\n"
      "v_mfma_f32_16x16x32_bf16 a[60:63], v[124:127], a[172:175], a[60:63]\n"
      "v_cndmask_b32_dpp v94, v233, v78, vcc row_ror:8 row_mask:0xf bank_mask:0xf\n"
      "v_mov_b32_dpp v78, v233 row_ror:8 row_mask:0xf bank_mask:0xc\n"
      "s_waitcnt lgkmcnt(7)\n"
      "v_mfma_f32_16x16x32_bf16 a[64:67], v[112:115], a[176:179], a[64:67]\n"
      "s_waitcnt vmcnt(8)\n"
      "v_perm_b32 v67, v28, v24, v235\n"
      "v_perm_b32 v232, v28, v24, v236\n"
      "v_mfma_f32_16x16x32_bf16 a[68:71], v[116:119], a[176:179], a[68:71]\n"
      "ds_read_b128 a[144:147], v148\n"
      "v_mfma_f32_16x16x32_bf16 a[72:75], v[120:123], a[176:179], a[72:75]\n"
      "v_mfma_f32_16x16x32_bf16 a[76:79], v[124:127], a[176:179], a[76:79]\n"
      "v_cndmask_b32_dpp v83, v232, v67, vcc row_ror:8 row_mask:0xf bank_mask:0xf\n"
      "v_mov_b32_dpp v67, v232 row_ror:8 row_mask:0xf bank_mask:0xc\n"
      "s_waitcnt lgkmcnt(7)\n"
      "v_mfma_f32_16x16x32_bf16 a[80:83], v[112:115], a[180:183], a[80:83]\n"
      "v_perm_b32 v71, v29, v25, v235\n"
      "v_perm_b32 v233, v29, v25, v236\n"
      "v_mfma_f32_16x16x32_bf16 a[84:87], v[116:119], a[180:183], a[84:87]\n"
      "ds_read_b128 a[148:151], v149\n"
      "v_mfma_f32_16x16x32_bf16 a[88:91], v[120:123], a[180:183], a[88:91]\n"
      "v_mfma_f32_16x16x32_bf16 a[92:95], v[124:127], a[180:183], a[92:95]\n"
      "v_cndmask_b32_dpp v87, v233, v71, vcc row_ror:8 row_mask:0xf bank_mask:0xf\n"
      "v_mov_b32_dpp v71, v233 row_ror:8 row_mask:0xf bank_mask:0xc\n"
      "s_waitcnt lgkmcnt(7)\n"
      "v_mfma_f32_16x16x32_bf16 a[96:99], v[112:115], a[184:187], a[96:99]\n"
      "v_perm_b32 v75, v30, v26, v235\n"
      "v_perm_b32 v232, v30, v26, v236\n"
      "v_mfma_f32_16x16x32_bf16 a[100:103], v[116:119], a[184:187], a[100:103]\n"
      "ds_read_b128 a[152:155], v150\n"
      "v_mfma_f32_16x16x32_bf16 a[104:107], v[120:123], a[184:187], a[104:107]\n"
      "v_mfma_f32_16x16x32_bf16 a[108:111], v[124:127], a[184:187], a[108:111]\n"
      "v_cndmask_b32_dpp v91, v232, v75, vcc row_ror:8 row_mask:0xf bank_mask:0xf\n"
      "v_mov_b32_dpp v75, v232 row_ror:8 row_mask:0xf bank_mask:0xc\n"
      "s_waitcnt lgkmcnt(7)\n"
      "v_mfma_f32_16x16x32_bf16 a[112:115], v[112:115], a[188:191], a[112:115]\n"
      "v_perm_b32 v79, v31, v27, v235\n"
      "v_perm_b32 v233, v31, v27, v236\n"
      "v_mfma_f32_16x16x32_bf16 a[116:119], v[116:119], a[188:191], a[116:119]\n"
      "ds_read_b128 a[156:159], v151\n"
      "v_mfma_f32_16x16x32_bf16 a[120:123], v[120:123], a[188:191], a[120:123]\n"
      "v_mfma_f32_16x16x32_bf16 a[124:127], v[124:127], a[188:191], a[124:127]\n"
      "v_cndmask_b32_dpp v95, v233, v79, vcc row_ror:8 row_mask:0xf bank_mask:0xf\n"
      "v_mov_b32_dpp v79, v233 row_ror:8 row_mask:0xf bank_mask:0xc\n"
      "s_add_u32 s40, s40, 131072\n"
      "s_addc_u32 s41, s41, 0\n"
      "s_waitcnt lgkmcnt(7)\n"
      "v_mfma_f32_16x16x32_bf16 a[0:3], v[64:67], a[128:131], a[0:3]\n"
      "s_waitcnt vmcnt(6)\n"
      "v_perm_b32 v96, v36, v32, v235\n"
      "v_perm_b32 v232, v36, v32, v236\n"
      "v_mfma_f32_16x16x32_bf16 a[4:7], v[68:71], a[128:131], a[4:7]\n"
      "ds_read_b128 a[160:163], v196\n"
      "v_mfma_f32_16x16x32_bf16 a[8:11], v[72:75], a[128:131], a[8:11]\n"
      "global_load_dwordx4 v[0:3], v234, s[40:41] offset:-4096\n"
      "v_mfma_f32_16x16x32_bf16 a[12:15], v[76:79], a[128:131], a[12:15]\n"
      "v_cndmask_b32_dpp v112, v232, v96, vcc row_ror:8 row_mask:0xf bank_mask:0xf\n"
      "v_mov_b32_dpp v96, v232 row_ror:8 row_mask:0xf bank_mask:0xc\n"
      "s_waitcnt lgkmcnt(7)\n"
      "v_mfma_f32_16x16x32_bf16 a[16:19], v[64:67], a[132:135], a[16:19]\n"
      "v_perm_b32 v100, v37, v33, v235\n"
      "v_perm_b32 v233, v37, v33, v236\n"
      "v_mfma_f32_16x16x32_bf16 a[20:23], v[68:71], a[132:135], a[20:23]\n"
      "ds_read_b128 a[164:167], v197\n"
      "v_mfma_f32_16x16x32_bf16 a[24:27], v[72:75], a[132:135], a[24:27]\n"
      "global_load_dwordx4 v[4:7], v234, s[40:41] offset:-3072\n"
      "v_mfma_f32_16x16x32_bf16 a[28:31], v[76:79], a[132:135], a[28:31]\n"
      "v_cndmask_b32_dpp v116, v233, v100, vcc row_ror:8 row_mask:0xf bank_mask:0xf\n"
      "v_mov_b32_dpp v100, v233 row_ror:8 row_mask:0xf bank_mask:0xc\n"
      "s_waitcnt lgkmcnt(7)\n"
      "v_mfma_f32_16x16x32_bf16 a[32:35], v[64:67], a[136:139], a[32:35]\n"
      "v_perm_b32 v104, v38, v34, v235\n"
      "v_perm_b32 v232, v38, v34, v236\n"
      "v_mfma_f32_16x16x32_bf16 a[36:39], v[68:71], a[136:139], a[36:39]\n"
      "ds_read_b128 a[168:171], v198\n"
      "v_mfma_f32_16x16x32_bf16 a[40:43], v[72:75], a[136:139], a[40:43]\n"
      "global_load_dwordx4 v[8:11], v234, s[40:41] offset:-2048\n"
      "v_mfma_f32_16x16x32_bf16 a[44:47], v[76:79], a[136:139], a[44:47]\n"
      "v_cndmask_b32_dpp v120, v232, v104, vcc row_ror:8 row_mask:0xf bank_mask:0xf\n"
      "v_mov_b32_dpp v104, v232 row_ror:8 row_mask:0xf bank_mask:0xc\n"
      "s_waitcnt lgkmcnt(7)\n"
      "v_mfma_f32_16x16x32_bf16 a[48:51], v[64:67], a[140:143], a[48:51]\n"
      "v_perm_b32 v108, v39, v35, v235\n"
      "v_perm_b32 v233, v39, v35, v236\n"
      "v_mfma_f32_16x16x32_bf16 a[52:55], v[68:71], a[140:143], a[52:55]\n"
      "ds_read_b128 a[172:175], v199\n"
      "v_mfma_f32_16x16x32_bf16 a[56:59], v[72:75], a[140:143], a[56:59]\n"
      "global_load_dwordx4 v[12:15], v234, s[40:41] offset:-1024\n"
      "v_mfma_f32_16x16x32_bf16 a[60:63], v[76:79], a[140:143], a[60:63]\n"
      "v_cndmask_b32_dpp v124, v233, v108, vcc row_ror:8 row_mask:0xf bank_mask:0xf\n"
      "v_mov_b32_dpp v108, v233 row_ror:8 row_mask:0xf bank_mask:0xc\n"
      "s_waitcnt lgkmcnt(7)\n"
      "v_mfma_f32_16x16x32_bf16 a[64:67], v[64:67], a[144:147], a[64:67]\n"
      "s_waitcnt vmcnt(8)\n"
      "v_perm_b32 v97, v44, v40, v235\n"
      "v_perm_b32 v232, v44, v40, v236\n"
      "v_mfma_f32_16x16x32_bf16 a[68:71], v[68:71], a[144:147], a[68:71]\n"
      "ds_read_b128 a[176:179], v200\n"
      "v_mfma_f32_16x16x32_bf16 a[72:75], v[72:75], a[144:147], a[72:75]\n"
      "global_load_dwordx4 v[16:19], v234, s[40:41] offset:0\n"
      "v_mfma_f32_16x16x32_bf16 a[76:79], v[76:79], a[144:147], a[76:79]\n"
      "v_cndmask_b32_dpp v113, v232, v97, vcc row_ror:8 row_mask:0xf bank_mask:0xf\n"
      "v_mov_b32_dpp v97, v232 row_ror:8 row_mask:0xf bank_mask:0xc\n"
      "s_waitcnt lgkmcnt(7)\n"
      "v_mfma_f32_16x16x32_bf16 a[80:83], v[64:67], a[148:151], a[80:83]\n"
      "v_perm_b32 v101, v45, v41, v235\n"
      "v_perm_b32 v233, v45, v41, v236\n"
      "v_mfma_f32_16x16x32_bf16 a[84:87], v[68:71], a[148:151], a[84:87]\n"
      "ds_read_b128 a[180:183], v201\n"
      "v_mfma_f32_16x16x32_bf16 a[88:91], v[72:75], a[148:151], a[88:91]\n"
      "global_load_dwordx4 v[20:23], v234, s[40:41] offset:1024\n"
      "v_mfma_f32_16x16x32_bf16 a[92:95], v[76:79], a[148:151], a[92:95]\n"
      "v_cndmask_b32_dpp v117, v233, v101, vcc row_ror:8 row_mask:0xf bank_mask:0xf\n"
      "v_mov_b32_dpp v101, v233 row_ror:8 row_mask:0xf bank_mask:0xc\n"
      "s_waitcnt lgkmcnt(7)\n"
      "v_mfma_f32_16x16x32_bf16 a[96:99], v[64:67], a[152:155], a[96:99]\n"
      "v_perm_b32 v105, v46, v42, v235\n"
      "v_perm_b32 v232, v46, v42, v236\n"
      "v_mfma_f32_16x16x32_bf16 a[100:103], v[68:71], a[152:155], a[100:103]\n"
      "ds_read_b128 a[184:187], v202\n"
      "v_mfma_f32_16x16x32_bf16 a[104:107], v[72:75], a[152:155], a[104:107]\n"
      "global_load_dwordx4 v[24:27], v234, s[40:41] offset:2048\n"
      "v_mfma_f32_16x16x32_bf16 a[108:111], v[76:79], a[152:155], a[108:111]\n"
      "v_cndmask_b32_dpp v121, v232, v105, vcc row_ror:8 row_mask:0xf bank_mask:0xf\n"
      "v_mov_b32_dpp v105, v232 row_ror:8 row_mask:0xf bank_mask:0xc\n"
      "s_waitcnt lgkmcnt(7)\n"
      "v_mfma_f32_16x16x32_bf16 a[112:115], v[64:67], a[156:159], a[112:115]\n"
      "v_perm_b32 v109, v47, v43, v235\n"
      "v_perm_b32 v233, v47, v43, v236\n"
      "v_mfma_f32_16x16x32_bf16 a[116:119], v[68:71], a[156:159], a[116:119]\n"
      "ds_read_b128 a[188:191], v203\n"
      "v_mfma_f32_16x16x32_bf16 a[120:123], v[72:75], a[156:159], a[120:123]\n"
      "global_load_dwordx4 v[28:31], v234, s[40:41] offset:3072\n"
      "v_mfma_f32_16x16x32_bf16 a[124:127], v[76:79], a[156:159], a[124:127]\n"
      "v_cndmask_b32_dpp v125, v233, v109, vcc row_ror:8 row_mask:0xf bank_mask:0xf\n"
      "v_mov_b32_dpp v109, v233 row_ror:8 row_mask:0xf bank_mask:0xc\n"
      "s_waitcnt lgkmcnt(7)\n"
      "v_mfma_f32_16x16x32_bf16 a[0:3], v[80:83], a[160:163], a[0:3]\n"
      "s_waitcnt vmcnt(10)\n"
      "v_perm_b32 v98, v52, v48, v235\n"
      "v_perm_b32 v232, v52, v48, v236\n"
      "v_mfma_f32_16x16x32_bf16 a[4:7], v[84:87], a[160:163], a[4:7]\n"
      "ds_read_b128 a[128:131], v152\n"
      "v_mfma_f32_16x16x32_bf16 a[8:11], v[88:91], a[160:163], a[8:11]\n"
      "v_mfma_f32_16x16x32_bf16 a[12:15], v[92:95], a[160:163], a[12:15]\n"
      "v_cndmask_b32_dpp v114, v232, v98, vcc row_ror:8 row_mask:0xf bank_mask:0xf\n"
      "v_mov_b32_dpp v98, v232 row_ror:8 row_mask:0xf bank_mask:0xc\n"
      "s_waitcnt lgkmcnt(7)\n"
      "v_mfma_f32_16x16x32_bf16 a[16:19], v[80:83], a[164:167], a[16:19]\n"
      "v_perm_b32 v102, v53, v49, v235\n"
      "v_perm_b32 v233, v53, v49, v236\n"
      "v_mfma_f32_16x16x32_bf16 a[20:23], v[84:87], a[164:167], a[20:23]\n"
      "ds_read_b128 a[132:135], v153\n"
      "v_mfma_f32_16x16x32_bf16 a[24:27], v[88:91], a[164:167], a[24:27]\n"
      "v_mfma_f32_16x16x32_bf16 a[28:31], v[92:95], a[164:167], a[28:31]\n"
      "v_cndmask_b32_dpp v118, v233, v102, vcc row_ror:8 row_mask:0xf bank_mask:0xf\n"
      "v_mov_b32_dpp v102, v233 row_ror:8 row_mask:0xf bank_mask:0xc\n"
      "s_waitcnt lgkmcnt(7)\n"
      "v_mfma_f32_16x16x32_bf16 a[32:35], v[80:83], a[168:171], a[32:35]\n"
      "v_perm_b32 v106, v54, v50, v235\n"
      "v_perm_b32 v232, v54, v50, v236\n"
      "v_mfma_f32_16x16x32_bf16 a[36:39], v[84:87], a[168:171], a[36:39]\n"
      "ds_read_b128 a[136:139], v154\n"
      "v_mfma_f32_16x16x32_bf16 a[40:43], v[88:91], a[168:171], a[40:43]\n"
      "v_mfma_f32_16x16x32_bf16 a[44:47], v[92:95], a[168:171], a[44:47]\n"
      "v_cndmask_b32_dpp v122, v232, v106, vcc row_ror:8 row_mask:0xf bank_mask:0xf\n"
      "v_mov_b32_dpp v106, v232 row_ror:8 row_mask:0xf bank_mask:0xc\n"
      "s_waitcnt lgkmcnt(7)\n"
      "v_mfma_f32_16x16x32_bf16 a[48:51], v[80:83], a[172:175], a[48:51]\n"
      "v_perm_b32 v110, v55, v51, v235\n"
      "v_perm_b32 v233, v55, v51, v236\n"
      "v_mfma_f32_16x16x32_bf16 a[52:55], v[84:87], a[172:175], a[52:55]\n"
      "ds_read_b128 a[140:143], v155\n"
      "v_mfma_f32_16x16x32_bf16 a[56:59], v[88:91], a[172:175], a[56:59]\n"
      "v_mfma_f32_16x16x32_bf16 a[60:63], v[92:95], a[172:175], a[60:63]\n"
      "v_cndmask_b32_dpp v126, v233, v110, vcc row_ror:8 row_mask:0xf bank_mask:0xf\n"
      "v_mov_b32_dpp v110, v233 row_ror:8 row_mask:0xf bank_mask:0xc\n"
      "s_waitcnt lgkmcnt(7)\n"
      "v_mfma_f32_16x16x32_bf16 a[64:67], v[80:83], a[176:179], a[64:67]\n"
      "s_waitcnt vmcnt(8)\n"
      "v_perm_b32 v99, v60, v56, v235\n"
      "v_perm_b32 v232, v60, v56, v236\n"
      "v_mfma_f32_16x16x32_bf16 a[68:71], v[84:87], a[176:179], a[68:71]\n"
      "ds_read_b128 a[144:147], v156\n"
      "v_mfma_f32_16x16x32_bf16 a[72:75], v[88:91], a[176:179], a[72:75]\n"
      "v_mfma_f32_16x16x32_bf16 a[76:79], v[92:95], a[176:179], a[76:79]\n"
      "v_cndmask_b32_dpp v115, v232, v99, vcc row_ror:8 row_mask:0xf bank_mask:0xf\n"
      "v_mov_b32_dpp v99, v232 row_ror:8 row_mask:0xf bank_mask:0xc\n"
      "s_waitcnt lgkmcnt(7)\n"
      "v_mfma_f32_16x16x32_bf16 a[80:83], v[80:83], a[180:183], a[80:83]\n"
      "v_perm_b32 v103, v61, v57, v235\n"
      "v_perm_b32 v233, v61, v57, v236\n"
      "v_mfma_f32_16x16x32_bf16 a[84:87], v[84:87], a[180:183], a[84:87]\n"
      "ds_read_b128 a[148:151], v157\n"
      "v_mfma_f32_16x16x32_bf16 a[88:91], v[88:91], a[180:183], a[88:91]\n"
      "v_mfma_f32_16x16x32_bf16 a[92:95], v[92:95], a[180:183], a[92:95]\n"
      "v_cndmask_b32_dpp v119, v233, v103, vcc row_ror:8 row_mask:0xf bank_mask:0xf\n"
      "v_mov_b32_dpp v103, v233 row_ror:8 row_mask:0xf bank_mask:0xc\n"
      "s_waitcnt lgkmcnt(7)\n"
      "v_mfma_f32_16x16x32_bf16 a[96:99], v[80:83], a[184:187], a[96:99]\n"
      "v_perm_b32 v107, v62, v58, v235\n"
      "v_perm_b32 v232, v62, v58, v236\n"
      "v_mfma_f32_16x16x32_bf16 a[100:103], v[84:87], a[184:187], a[100:103]\n"
      "ds_read_b128 a[152:155], v158\n"
      "v_mfma_f32_16x16x32_bf16 a[104:107], v[88:91], a[184:187], a[104:107]\n"
      "v_mfma_f32_16x16x32_bf16 a[108:111], v[92:95], a[184:187], a[108:111]\n"
      "v_cndmask_b32_dpp v123, v232, v107, vcc row_ror:8 row_mask:0xf bank_mask:0xf\n"
      "v_mov_b32_dpp v107, v232 row_ror:8 row_mask:0xf bank_mask:0xc\n"
      "s_waitcnt lgkmcnt(7)\n"
      "v_mfma_f32_16x16x32_bf16 a[112:115], v[80:83], a[188:191], a[112:115]\n"
      "v_perm_b32 v111, v63, v59, v235\n"
      "v_perm_b32 v233, v63, v59, v236\n"
      "v_mfma_f32_16x16x32_bf16 a[116:119], v[84:87], a[188:191], a[116:119]\n"
      "ds_read_b128 a[156:159], v159\n"
      "v_mfma_f32_16x16x32_bf16 a[120:123], v[88:91], a[188:191], a[120:123]\n"
      "v_mfma_f32_16x16x32_bf16 a[124:127], v[92:95], a[188:191], a[124:127]\n"
      "v_cndmask_b32_dpp v127, v233, v111, vcc row_ror:8 row_mask:0xf bank_mask:0xf\n"
      "v_mov_b32_dpp v111, v233 row_ror:8 row_mask:0xf bank_mask:0xc\n"
      "s_add_u32 s40, s40, 131072\n"
      "s_addc_u32 s41, s41, 0\n"
      "s_waitcnt lgkmcnt(7)\n"
      "v_mfma_f32_16x16x32_bf16 a[0:3], v[96:99], a[128:131], a[0:3]\n"
      "s_waitcnt vmcnt(6)\n"
      "v_perm_b32 v64, v4, v0, v235\n"
      "v_perm_b32 v232, v4, v0, v236\n"
      "v_mfma_f32_16x16x32_bf16 a[4:7], v[100:103], a[128:131], a[4:7]\n"
      "ds_read_b128 a[160:163], v204\n"
      "v_mfma_f32_16x16x32_bf16 a[8:11], v[104:107], a[128:131], a[8:11]\n"
      "global_load_dwordx4 v[32:35], v234, s[40:41] offset:-4096\n"
      "v_mfma_f32_16x16x32_bf16 a[12:15], v[108:111], a[128:131], a[12:15]\n"
      "v_cndmask_b32_dpp v80, v232, v64, vcc row_ror:8 row_mask:0xf bank_mask:0xf\n"
      "v_mov_b32_dpp v64, v232 row_ror:8 row_mask:0xf bank_mask:0xc\n"
      "s_waitcnt lgkmcnt(7)\n"
      "v_mfma_f32_16x16x32_bf16 a[16:19], v[96:99], a[132:135], a[16:19]\n"
      "v_perm_b32 v68, v5, v1, v235\n"
      "v_perm_b32 v233, v5, v1, v236\n"
      "v_mfma_f32_16x16x32_bf16 a[20:23], v[100:103], a[132:135], a[20:23]\n"
      "ds_read_b128 a[164:167], v205\n"
      "v_mfma_f32_16x16x32_bf16 a[24:27], v[104:107], a[132:135], a[24:27]\n"
      "global_load_dwordx4 v[36:39], v234, s[40:41] offset:-3072\n"
      "v_mfma_f32_16x16x32_bf16 a[28:31], v[108:111], a[132:135], a[28:31]\n"
      "v_cndmask_b32_dpp v84, v233, v68, vcc row_ror:8 row_mask:0xf bank_mask:0xf\n"
      "v_mov_b32_dpp v68, v233 row_ror:8 row_mask:0xf bank_mask:0xc\n"
      "s_waitcnt lgkmcnt(7)\n"
      "v_mfma_f32_16x16x32_bf16 a[32:35], v[96:99], a[136:139], a[32:35]\n"
      "v_perm_b32 v72, v6, v2, v235\n"
      "v_perm_b32 v232, v6, v2, v236\n"
      "v_mfma_f32_16x16x32_bf16 a[36:39], v[100:103], a[136:139], a[36:39]\n"
      "ds_read_b128 a[168:171], v206\n"
      "v_mfma_f32_16x16x32_bf16 a[40:43], v[104:107], a[136:139], a[40:43]\n"
      "global_load_dwordx4 v[40:43], v234, s[40:41] offset:-2048\n"
      "v_mfma_f32_16x16x32_bf16 a[44:47], v[108:111], a[136:139], a[44:47]\n"
      "v_cndmask_b32_dpp v88, v232, v72, vcc row_ror:8 row_mask:0xf bank_mask:0xf\n"
      "v_mov_b32_dpp v72, v232 row_ror:8 row_mask:0xf bank_mask:0xc\n"
      "s_waitcnt lgkmcnt(7)\n"
      "v_mfma_f32_16x16x32_bf16 a[48:51], v[96:99], a[140:143], a[48:51]\n"
      "v_perm_b32 v76, v7, v3, v235\n"
      "v_perm_b32 v233, v7, v3, v236\n"
      "v_mfma_f32_16x16x32_bf16 a[52:55], v[100:103], a[140:143], a[52:55]\n"
      "ds_read_b128 a[172:175], v207\n"
      "v_mfma_f32_16x16x32_bf16 a[56:59], v[104:107], a[140:143], a[56:59]\n"
      "global_load_dwordx4 v[44:47], v234, s[40:41] offset:-1024\n"
      "v_mfma_f32_16x16x32_bf16 a[60:63], v[108:111], a[140:143], a[60:63]\n"
      "v_cndmask_b32_dpp v92, v233, v76, vcc row_ror:8 row_mask:0xf bank_mask:0xf\n"
      "v_mov_b32_dpp v76, v233 row_ror:8 row_mask:0xf bank_mask:0xc\n"
      "s_waitcnt lgkmcnt(7)\n"
      "v_mfma_f32_16x16x32_bf16 a[64:67], v[96:99], a[144:147], a[64:67]\n"
      "s_waitcnt vmcnt(8)\n"
      "v_perm_b32 v65, v12, v8, v235\n"
      "v_perm_b32 v232, v12, v8, v236\n"
      "v_mfma_f32_16x16x32_bf16 a[68:71], v[100:103], a[144:147], a[68:71]\n"
      "ds_read_b128 a[176:179], v208\n"
      "v_mfma_f32_16x16x32_bf16 a[72:75], v[104:107], a[144:147], a[72:75]\n"
      "global_load_dwordx4 v[48:51], v234, s[40:41] offset:0\n"
      "v_mfma_f32_16x16x32_bf16 a[76:79], v[108:111], a[144:147], a[76:79]\n"
      "v_cndmask_b32_dpp v81, v232, v65, vcc row_ror:8 row_mask:0xf bank_mask:0xf\n"
      "v_mov_b32_dpp v65, v232 row_ror:8 row_mask:0xf bank_mask:0xc\n"
      "s_waitcnt lgkmcnt(7)\n"
      "v_mfma_f32_16x16x32_bf16 a[80:83], v[96:99], a[148:151], a[80:83]\n"
      "v_perm_b32 v69, v13, v9, v235\n"
      "v_perm_b32 v233, v13, v9, v236\n"
      "v_mfma_f32_16x16x32_bf16 a[84:87], v[100:103], a[148:151], a[84:87]\n"
      "ds_read_b128 a[180:183], v209\n"
      "v_mfma_f32_16x16x32_bf16 a[88:91], v[104:107], a[148:151], a[88:91]\n"
      "global_load_dwordx4 v[52:55], v234, s[40:41] offset:1024\n"
      "v_mfma_f32_16x16x32_bf16 a[92:95], v[108:111], a[148:151], a[92:95]\n"
      "v_cndmask_b32_dpp v85, v233, v69, vcc row_ror:8 row_mask:0xf bank_mask:0xf\n"
      "v_mov_b32_dpp v69, v233 row_ror:8 row_mask:0xf bank_mask:0xc\n"
      "s_waitcnt lgkmcnt(7)\n"
      "v_mfma_f32_16x16x32_bf16 a[96:99], v[96:99], a[152:155], a[96:99]\n"
      "v_perm_b32 v73, v14, v10, v235\n"
      "v_perm_b32 v232, v14, v10, v236\n"
      "v_mfma_f32_16x16x32_bf16 a[100:103], v[100:103], a[152:155], a[100:103]\n"
      "ds_read_b128 a[184:187], v210\n"
      "v_mfma_f32_16x16x32_bf16 a[104:107], v[104:107], a[152:155], a[104:107]\n"
      "global_load_dwordx4 v[56:59], v234, s[40:41] offset:2048\n"
      "v_mfma_f32_16x16x32_bf16 a[108:111], v[108:111], a[152:155], a[108:111]\n"
      "v_cndmask_b32_dpp v89, v232, v73, vcc row_ror:8 row_mask:0xf bank_mask:0xf\n"
      "v_mov_b32_dpp v73, v232 row_ror:8 row_mask:0xf bank_mask:0xc\n"
      "s_waitcnt lgkmcnt(7)\n"
      "v_mfma_f32_16x16x32_bf16 a[112:115], v[96:99], a[156:159], a[112:115]\n"
      "v_perm_b32 v77, v15, v11, v235\n"
      "v_perm_b32 v233, v15, v11, v236\n"
      "v_mfma_f32_16x16x32_bf16 a[116:119], v[100:103], a[156:159], a[116:119]\n"
      "ds_read_b128 a[188:191], v211\n"
      "v_mfma_f32_16x16x32_bf16 a[120:123], v[104:107], a[156:159], a[120:123]\n"
      "global_load_dwordx4 v[60:63], v234, s[40:41] offset:3072\n"
      "v_mfma_f32_16x16x32_bf16 a[124:127], v[108:111], a[156:159], a[124:127]\n"
      "v_cndmask_b32_dpp v93, v233, v77, vcc row_ror:8 row_mask:0xf bank_mask:0xf\n"
      "v_mov_b32_dpp v77, v233 row_ror:8 row_mask:0xf bank_mask:0xc\n"
      "s_waitcnt lgkmcnt(7)\n"
      "v_mfma_f32_16x16x32_bf16 a[0:3], v[112:115], a[160:163], a[0:3]\n"
      "s_waitcnt vmcnt(10)\n"
      "v_perm_b32 v66, v20, v16, v235\n"
      "v_perm_b32 v232, v20, v16, v236\n"
      "v_mfma_f32_16x16x32_bf16 a[4:7], v[116:119], a[160:163], a[4:7]\n"
      "ds_read_b128 a[128:131], v160\n"
      "v_mfma_f32_16x16x32_bf16 a[8:11], v[120:123], a[160:163], a[8:11]\n"
      "v_mfma_f32_16x16x32_bf16 a[12:15], v[124:127], a[160:163], a[12:15]\n"
      "v_cndmask_b32_dpp v82, v232, v66, vcc row_ror:8 row_mask:0xf bank_mask:0xf\n"
      "v_mov_b32_dpp v66, v232 row_ror:8 row_mask:0xf bank_mask:0xc\n"
      "s_waitcnt lgkmcnt(7)\n"
      "v_mfma_f32_16x16x32_bf16 a[16:19], v[112:115], a[164:167], a[16:19]\n"
      "v_perm_b32 v70, v21, v17, v235\n"
      "v_perm_b32 v233, v21, v17, v236\n"
      "v_mfma_f32_16x16x32_bf16 a[20:23], v[116:119], a[164:167], a[20:23]\n"
      "ds_read_b128 a[132:135], v161\n"
      "v_mfma_f32_16x16x32_bf16 a[24:27], v[120:123], a[164:167], a[24:27]\n"
      "v_mfma_f32_16x16x32_bf16 a[28:31], v[124:127], a[164:167], a[28:31]\n"
      "v_cndmask_b32_dpp v86, v233, v70, vcc row_ror:8 row_mask:0xf bank_mask:0xf\n"
      "v_mov_b32_dpp v70, v233 row_ror:8 row_mask:0xf bank_mask:0xc\n"
      "s_waitcnt lgkmcnt(7)\n"
      "v_mfma_f32_16x16x32_bf16 a[32:35], v[112:115], a[168:171], a[32:35]\n"
      "v_perm_b32 v74, v22, v18, v235\n"
      "v_perm_b32 v232, v22, v18, v236\n"
      "v_mfma_f32_16x16x32_bf16 a[36:39], v[116:119], a[168:171], a[36:39]\n"
      "ds_read_b128 a[136:139], v162\n"
      "v_mfma_f32_16x16x32_bf16 a[40:43], v[120:123], a[168:171], a[40:43]\n"
      "v_mfma_f32_16x16x32_bf16 a[44:47], v[124:127], a[168:171], a[44:47]\n"
      "v_cndmask_b32_dpp v90, v232, v74, vcc row_ror:8 row_mask:0xf bank_mask:0xf\n"
      "v_mov_b32_dpp v74, v232 row_ror:8 row_mask:0xf bank_mask:0xc\n"
      "s_waitcnt lgkmcnt(7)\n"
      "v_mfma_f32_16x16x32_bf16 a[48:51], v[112:115], a[172:175], a[48:51]\n"
      "v_perm_b32 v78, v23, v19, v235\n"
      "v_perm_b32 v233, v23, v19, v236\n"
      "v_mfma_f32_16x16x32_bf16 a[52:55], v[116:119], a[172:175], a[52:55]\n"
      "ds_read_b128 a[140:143], v163\n"
      "v_mfma_f32_16x16x32_bf16 a[56:59], v[120:123], a[172:175], a[56:59]\n"
      "v_mfma_f32_16x16x32_bf16 a[60:63], v[124:127], a[172:175], a[60:63]\n"
      "v_cndmask_b32_dpp v94, v233, v78, vcc row_ror:8 row_mask:0xf bank_mask:0xf\n"
      "v_mov_b32_dpp v78, v233 row_ror:8 row_mask:0xf bank_mask:0xc\n"
      "s_waitcnt lgkmcnt(7)\n"
      "v_mfma_f32_16x16x32_bf16 a[64:67], v[112:115], a[176:179], a[64:67]\n"
      "s_waitcnt vmcnt(8)\n"
      "v_perm_b32 v67, v28, v24, v235\n"
      "v_perm_b32 v232, v28, v24, v236\n"
      "v_mfma_f32_16x16x32_bf16 a[68:71], v[116:119], a[176:179], a[68:71]\n"
      "ds_read_b128 a[144:147], v164\n"
      "v_mfma_f32_16x16x32_bf16 a[72:75], v[120:123], a[176:179], a[72:75]\n"
      "v_mfma_f32_16x16x32_bf16 a[76:79], v[124:127], a[176:179], a[76:79]\n"
      "v_cndmask_b32_dpp v83, v232, v67, vcc row_ror:8 row_mask:0xf bank_mask:0xf\n"
      "v_mov_b32_dpp v67, v232 row_ror:8 row_mask:0xf bank_mask:0xc\n"
      "s_waitcnt lgkmcnt(7)\n"
      "v_mfma_f32_16x16x32_bf16 a[80:83], v[112:115], a[180:183], a[80:83]\n"
      "v_perm_b32 v71, v29, v25, v235\n"
      "v_perm_b32 v233, v29, v25, v236\n"
      "v_mfma_f32_16x16x32_bf16 a[84:87], v[116:119], a[180:183], a[84:87]\n"
      "ds_read_b128 a[148:151], v165\n"
      "v_mfma_f32_16x16x32_bf16 a[88:91], v[120:123], a[180:183], a[88:91]\n"
      "v_mfma_f32_16x16x32_bf16 a[92:95], v[124:127], a[180:183], a[92:95]\n"
      "v_cndmask_b32_dpp v87, v233, v71, vcc row_ror:8 row_mask:0xf bank_mask:0xf\n"
      "v_mov_b32_dpp v71, v233 row_ror:8 row_mask:0xf bank_mask:0xc\n"
      "s_waitcnt lgkmcnt(7)\n"
      "v_mfma_f32_16x16x32_bf16 a[96:99], v[112:115], a[184:187], a[96:99]\n"
      "v_perm_b32 v75, v30, v26, v235\n"
      "v_perm_b32 v232, v30, v26, v236\n"
      "v_mfma_f32_16x16x32_bf16 a[100:103], v[116:119], a[184:187], a[100:103]\n"
      "ds_read_b128 a[152:155], v166\n"
      "v_mfma_f32_16x16x32_bf16 a[104:107], v[120:123], a[184:187], a[104:107]\n"
      "v_mfma_f32_16x16x32_bf16 a[108:111], v[124:127], a[184:187], a[108:111]\n"
      "v_cndmask_b32_dpp v91, v232, v75, vcc row_ror:8 row_mask:0xf bank_mask:0xf\n"
      "v_mov_b32_dpp v75, v232 row_ror:8 row_mask:0xf bank_mask:0xc\n"
      "s_waitcnt lgkmcnt(7)\n"
      "v_mfma_f32_16x16x32_bf16 a[112:115], v[112:115], a[188:191], a[112:115]\n"
      "v_perm_b32 v79, v31, v27, v235\n"
      "v_perm_b32 v233, v31, v27, v236\n"
      "v_mfma_f32_16x16x32_bf16 a[116:119], v[116:119], a[188:191], a[116:119]\n"
      "ds_read_b128 a[156:159], v167\n"
      "v_mfma_f32_16x16x32_bf16 a[120:123], v[120:123], a[188:191], a[120:123]\n"
      "v_mfma_f32_16x16x32_bf16 a[124:127], v[124:127], a[188:191], a[124:127]\n"
      "v_cndmask_b32_dpp v95, v233, v79, vcc row_ror:8 row_mask:0xf bank_mask:0xf\n"
      "v_mov_b32_dpp v79, v233 row_ror:8 row_mask:0xf bank_mask:0xc\n"
      "s_waitcnt lgkmcnt(7)\n"
      "v_mfma_f32_16x16x32_bf16 a[0:3], v[64:67], a[128:131], a[0:3]\n"
      "s_waitcnt vmcnt(6)\n"
      "v_perm_b32 v96, v36, v32, v235\n"
      "v_perm_b32 v232, v36, v32, v236\n"
      "v_mfma_f32_16x16x32_bf16 a[4:7], v[68:71], a[128:131], a[4:7]\n"
      "ds_read_b128 a[160:163], v212\n"
      "v_mfma_f32_16x16x32_bf16 a[8:11], v[72:75], a[128:131], a[8:11]\n"
      "global_load_dwordx4 v[0:3], v234, s[44:45] offset:-4096\n"
      "v_mfma_f32_16x16x32_bf16 a[12:15], v[76:79], a[128:131], a[12:15]\n"
      "v_cndmask_b32_dpp v112, v232, v96, vcc row_ror:8 row_mask:0xf bank_mask:0xf\n"
      "v_mov_b32_dpp v96, v232 row_ror:8 row_mask:0xf bank_mask:0xc\n"
      "s_waitcnt lgkmcnt(7)\n"
      "v_mfma_f32_16x16x32_bf16 a[16:19], v[64:67], a[132:135], a[16:19]\n"
      "v_perm_b32 v100, v37, v33, v235\n"
      "v_perm_b32 v233, v37, v33, v236\n"
      "v_mfma_f32_16x16x32_bf16 a[20:23], v[68:71], a[132:135], a[20:23]\n"
      "ds_read_b128 a[164:167], v213\n"
      "v_mfma_f32_16x16x32_bf16 a[24:27], v[72:75], a[132:135], a[24:27]\n"
      "global_load_dwordx4 v[4:7], v234, s[44:45] offset:-3072\n"
      "v_mfma_f32_16x16x32_bf16 a[28:31], v[76:79], a[132:135], a[28:31]\n"
      "v_cndmask_b32_dpp v116, v233, v100, vcc row_ror:8 row_mask:0xf bank_mask:0xf\n"
      "v_mov_b32_dpp v100, v233 row_ror:8 row_mask:0xf bank_mask:0xc\n"
      "s_waitcnt lgkmcnt(7)\n"
      "v_mfma_f32_16x16x32_bf16 a[32:35], v[64:67], a[136:139], a[32:35]\n"
      "v_perm_b32 v104, v38, v34, v235\n"
      "v_perm_b32 v232, v38, v34, v236\n"
      "v_mfma_f32_16x16x32_bf16 a[36:39], v[68:71], a[136:139], a[36:39]\n"
      "ds_read_b128 a[168:171], v214\n"
      "v_mfma_f32_16x16x32_bf16 a[40:43], v[72:75], a[136:139], a[40:43]\n"
      "global_load_dwordx4 v[8:11], v234, s[44:45] offset:-2048\n"
      "v_mfma_f32_16x16x32_bf16 a[44:47], v[76:79], a[136:139], a[44:47]\n"
      "v_cndmask_b32_dpp v120, v232, v104, vcc row_ror:8 row_mask:0xf bank_mask:0xf\n"
      "v_mov_b32_dpp v104, v232 row_ror:8 row_mask:0xf bank_mask:0xc\n"
      "s_waitcnt lgkmcnt(7)\n"
      "v_mfma_f32_16x16x32_bf16 a[48:51], v[64:67], a[140:143], a[48:51]\n"
      "v_perm_b32 v108, v39, v35, v235\n"
      "v_perm_b32 v233, v39, v35, v236\n"
      "v_mfma_f32_16x16x32_bf16 a[52:55], v[68:71], a[140:143], a[52:55]\n"
      "ds_read_b128 a[172:175], v215\n"
      "v_mfma_f32_16x16x32_bf16 a[56:59], v[72:75], a[140:143], a[56:59]\n"
      "global_load_dwordx4 v[12:15], v234, s[44:45] offset:-1024\n"
      "v_mfma_f32_16x16x32_bf16 a[60:63], v[76:79], a[140:143], a[60:63]\n"
      "v_cndmask_b32_dpp v124, v233, v108, vcc row_ror:8 row_mask:0xf bank_mask:0xf\n"
      "v_mov_b32_dpp v108, v233 row_ror:8 row_mask:0xf bank_mask:0xc\n"
      "s_waitcnt lgkmcnt(7)\n"
      "v_mfma_f32_16x16x32_bf16 a[64:67], v[64:67], a[144:147], a[64:67]\n"
      "s_waitcnt vmcnt(8)\n"
      "v_perm_b32 v97, v44, v40, v235\n"
      "v_perm_b32 v232, v44, v40, v236\n"
      "v_mfma_f32_16x16x32_bf16 a[68:71], v[68:71], a[144:147], a[68:71]\n"
      "ds_read_b128 a[176:179], v216\n"
      "v_mfma_f32_16x16x32_bf16 a[72:75], v[72:75], a[144:147], a[72:75]\n"
      "global_load_dwordx4 v[16:19], v234, s[44:45] offset:0\n"
      "v_mfma_f32_16x16x32_bf16 a[76:79], v[76:79], a[144:147], a[76:79]\n"
      "v_cndmask_b32_dpp v113, v232, v97, vcc row_ror:8 row_mask:0xf bank_mask:0xf\n"
      "v_mov_b32_dpp v97, v232 row_ror:8 row_mask:0xf bank_mask:0xc\n"
      "s_waitcnt lgkmcnt(7)\n"
      "v_mfma_f32_16x16x32_bf16 a[80:83], v[64:67], a[148:151], a[80:83]\n"
      "v_perm_b32 v101, v45, v41, v235\n"
      "v_perm_b32 v233, v45, v41, v236\n"
      "v_mfma_f32_16x16x32_bf16 a[84:87], v[68:71], a[148:151], a[84:87]\n"
      "ds_read_b128 a[180:183], v217\n"
      "v_mfma_f32_16x16x32_bf16 a[88:91], v[72:75], a[148:151], a[88:91]\n"
      "global_load_dwordx4 v[20:23], v234, s[44:45] offset:1024\n"
      "v_mfma_f32_16x16x32_bf16 a[92:95], v[76:79], a[148:151], a[92:95]\n"
      "v_cndmask_b32_dpp v117, v233, v101, vcc row_ror:8 row_mask:0xf bank_mask:0xf\n"
      "v_mov_b32_dpp v101, v233 row_ror:8 row_mask:0xf bank_mask:0xc\n"
      "s_waitcnt lgkmcnt(7)\n"
      "v_mfma_f32_16x16x32_bf16 a[96:99], v[64:67], a[152:155], a[96:99]\n"
      "v_perm_b32 v105, v46, v42, v235\n"
      "v_perm_b32 v232, v46, v42, v236\n"
      "v_mfma_f32_16x16x32_bf16 a[100:103], v[68:71], a[152:155], a[100:103]\n"
      "ds_read_b128 a[184:187], v218\n"
      "v_mfma_f32_16x16x32_bf16 a[104:107], v[72:75], a[152:155], a[104:107]\n"
      "global_load_dwordx4 v[24:27], v234, s[44:45] offset:2048\n"
      "v_mfma_f32_16x16x32_bf16 a[108:111], v[76:79], a[152:155], a[108:111]\n"
      "v_cndmask_b32_dpp v121, v232, v105, vcc row_ror:8 row_mask:0xf bank_mask:0xf\n"
      "v_mov_b32_dpp v105, v232 row_ror:8 row_mask:0xf bank_mask:0xc\n"
      "s_waitcnt lgkmcnt(7)\n"
      "v_mfma_f32_16x16x32_bf16 a[112:115], v[64:67], a[156:159], a[112:115]\n"
      "v_perm_b32 v109, v47, v43, v235\n"
      "v_perm_b32 v233, v47, v43, v236\n"
      "v_mfma_f32_16x16x32_bf16 a[116:119], v[68:71], a[156:159], a[116:119]\n"
      "ds_read_b128 a[188:191], v219\n"
      "v_mfma_f32_16x16x32_bf16 a[120:123], v[72:75], a[156:159], a[120:123]\n"
      "global_load_dwordx4 v[28:31], v234, s[44:45] offset:3072\n"
      "v_mfma_f32_16x16x32_bf16 a[124:127], v[76:79], a[156:159], a[124:127]\n"
      "v_cndmask_b32_dpp v125, v233, v109, vcc row_ror:8 row_mask:0xf bank_mask:0xf\n"
      "v_mov_b32_dpp v109, v233 row_ror:8 row_mask:0xf bank_mask:0xc\n"
      "s_waitcnt lgkmcnt(7)\n"
      "v_mfma_f32_16x16x32_bf16 a[0:3], v[80:83], a[160:163], a[0:3]\n"
      "s_waitcnt vmcnt(10)\n"
      "v_perm_b32 v98, v52, v48, v235\n"
      "v_perm_b32 v232, v52, v48, v236\n"
      "v_mfma_f32_16x16x32_bf16 a[4:7], v[84:87], a[160:163], a[4:7]\n"
      "ds_read_b128 a[128:131], v168\n"
      "v_mfma_f32_16x16x32_bf16 a[8:11], v[88:91], a[160:163], a[8:11]\n"
      "v_mfma_f32_16x16x32_bf16 a[12:15], v[92:95], a[160:163], a[12:15]\n"
      "v_cndmask_b32_dpp v114, v232, v98, vcc row_ror:8 row_mask:0xf bank_mask:0xf\n"
      "v_mov_b32_dpp v98, v232 row_ror:8 row_mask:0xf bank_mask:0xc\n"
      "s_waitcnt lgkmcnt(7)\n"
      "v_mfma_f32_16x16x32_bf16 a[16:19], v[80:83], a[164:167], a[16:19]\n"
      "v_perm_b32 v102, v53, v49, v235\n"
      "v_perm_b32 v233, v53, v49, v236\n"
      "v_mfma_f32_16x16x32_bf16 a[20:23], v[84:87], a[164:167], a[20:23]\n"
      "ds_read_b128 a[132:135], v169\n"
      "v_mfma_f32_16x16x32_bf16 a[24:27], v[88:91], a[164:167], a[24:27]\n"
      "v_mfma_f32_16x16x32_bf16 a[28:31], v[92:95], a[164:167], a[28:31]\n"
      "v_cndmask_b32_dpp v118, v233, v102, vcc row_ror:8 row_mask:0xf bank_mask:0xf\n"
      "v_mov_b32_dpp v102, v233 row_ror:8 row_mask:0xf bank_mask:0xc\n"
      "s_waitcnt lgkmcnt(7)\n"
      "v_mfma_f32_16x16x32_bf16 a[32:35], v[80:83], a[168:171], a[32:35]\n"
      "v_perm_b32 v106, v54, v50, v235\n"
      "v_perm_b32 v232, v54, v50, v236\n"
      "v_mfma_f32_16x16x32_bf16 a[36:39], v[84:87], a[168:171], a[36:39]\n"
      "ds_read_b128 a[136:139], v170\n"
      "v_mfma_f32_16x16x32_bf16 a[40:43], v[88:91], a[168:171], a[40:43]\n"
      "v_mfma_f32_16x16x32_bf16 a[44:47], v[92:95], a[168:171], a[44:47]\n"
      "v_cndmask_b32_dpp v122, v232, v106, vcc row_ror:8 row_mask:0xf bank_mask:0xf\n"
      "v_mov_b32_dpp v106, v232 row_ror:8 row_mask:0xf bank_mask:0xc\n"
      "s_waitcnt lgkmcnt(7)\n"
      "v_mfma_f32_16x16x32_bf16 a[48:51], v[80:83], a[172:175], a[48:51]\n"
      "v_perm_b32 v110, v55, v51, v235\n"
      "v_perm_b32 v233, v55, v51, v236\n"
      "v_mfma_f32_16x16x32_bf16 a[52:55], v[84:87], a[172:175], a[52:55]\n"
      "ds_read_b128 a[140:143], v171\n"
      "v_mfma_f32_16x16x32_bf16 a[56:59], v[88:91], a[172:175], a[56:59]\n"
      "v_mfma_f32_16x16x32_bf16 a[60:63], v[92:95], a[172:175], a[60:63]\n"
      "v_cndmask_b32_dpp v126, v233, v110, vcc row_ror:8 row_mask:0xf bank_mask:0xf\n"
      "v_mov_b32_dpp v110, v233 row_ror:8 row_mask:0xf bank_mask:0xc\n"
      "s_waitcnt lgkmcnt(7)\n"
      "v_mfma_f32_16x16x32_bf16 a[64:67], v[80:83], a[176:179], a[64:67]\n"
      "s_waitcnt vmcnt(8)\n"
      "v_perm_b32 v99, v60, v56, v235\n"
      "v_perm_b32 v232, v60, v56, v236\n"
      "v_mfma_f32_16x16x32_bf16 a[68:71], v[84:87], a[176:179], a[68:71]\n"
      "ds_read_b128 a[144:147], v172\n"
      "v_mfma_f32_16x16x32_bf16 a[72:75], v[88:91], a[176:179], a[72:75]\n"
      "v_mfma_f32_16x16x32_bf16 a[76:79], v[92:95], a[176:179], a[76:79]\n"
      "v_cndmask_b32_dpp v115, v232, v99, vcc row_ror:8 row_mask:0xf bank_mask:0xf\n"
      "v_mov_b32_dpp v99, v232 row_ror:8 row_mask:0xf bank_mask:0xc\n"
      "s_waitcnt lgkmcnt(7)\n"
      "v_mfma_f32_16x16x32_bf16 a[80:83], v[80:83], a[180:183], a[80:83]\n"
      "v_perm_b32 v103, v61, v57, v235\n"
      "v_perm_b32 v233, v61, v57, v236\n"
      "v_mfma_f32_16x16x32_bf16 a[84:87], v[84:87], a[180:183], a[84:87]\n"
      "ds_read_b128 a[148:151], v173\n"
      "v_mfma_f32_16x16x32_bf16 a[88:91], v[88:91], a[180:183], a[88:91]\n"
      "v_mfma_f32_16x16x32_bf16 a[92:95], v[92:95], a[180:183], a[92:95]\n"
      "v_cndmask_b32_dpp v119, v233, v103, vcc row_ror:8 row_mask:0xf bank_mask:0xf\n"
      "v_mov_b32_dpp v103, v233 row_ror:8 row_mask:0xf bank_mask:0xc\n"
      "s_waitcnt lgkmcnt(7)\n"
      "v_mfma_f32_16x16x32_bf16 a[96:99], v[80:83], a[184:187], a[96:99]\n"
      "v_perm_b32 v107, v62, v58, v235\n"
      "v_perm_b32 v232, v62, v58, v236\n"
      "v_mfma_f32_16x16x32_bf16 a[100:103], v[84:87], a[184:187], a[100:103]\n"
      "ds_read_b128 a[152:155], v174\n"
      "v_mfma_f32_16x16x32_bf16 a[104:107], v[88:91], a[184:187], a[104:107]\n"
      "v_mfma_f32_16x16x32_bf16 a[108:111], v[92:95], a[184:187], a[108:111]\n"
      "v_cndmask_b32_dpp v123, v232, v107, vcc row_ror:8 row_mask:0xf bank_mask:0xf\n"
      "v_mov_b32_dpp v107, v232 row_ror:8 row_mask:0xf bank_mask:0xc\n"
      "s_waitcnt lgkmcnt(7)\n"
      "v_mfma_f32_16x16x32_bf16 a[112:115], v[80:83], a[188:191], a[112:115]\n"
      "v_perm_b32 v111, v63, v59, v235\n"
      "v_perm_b32 v233, v63, v59, v236\n"
      "v_mfma_f32_16x16x32_bf16 a[116:119], v[84:87], a[188:191], a[116:119]\n"
      "ds_read_b128 a[156:159], v175\n"
      "v_mfma_f32_16x16x32_bf16 a[120:123], v[88:91], a[188:191], a[120:123]\n"
      "v_mfma_f32_16x16x32_bf16 a[124:127], v[92:95], a[188:191], a[124:127]\n"
      "v_cndmask_b32_dpp v127, v233, v111, vcc row_ror:8 row_mask:0xf bank_mask:0xf\n"
      "v_mov_b32_dpp v111, v233 row_ror:8 row_mask:0xf bank_mask:0xc\n"
      "s_waitcnt lgkmcnt(7)\n"
      "v_mfma_f32_16x16x32_bf16 a[0:3], v[96:99], a[128:131], a[0:3]\n"
      "s_waitcnt vmcnt(6)\n"
      "v_perm_b32 v64, v4, v0, v235\n"
      "v_perm_b32 v232, v4, v0, v236\n"
      "v_mfma_f32_16x16x32_bf16 a[4:7], v[100:103], a[128:131], a[4:7]\n"
      "ds_read_b128 a[160:163], v220\n"
      "v_mfma_f32_16x16x32_bf16 a[8:11], v[104:107], a[128:131], a[8:11]\n"
      "global_load_dwordx4 v[32:35], v234, s[46:47] offset:-4096\n"
      "v_mfma_f32_16x16x32_bf16 a[12:15], v[108:111], a[128:131], a[12:15]\n"
      "v_cndmask_b32_dpp v80, v232, v64, vcc row_ror:8 row_mask:0xf bank_mask:0xf\n"
      "v_mov_b32_dpp v64, v232 row_ror:8 row_mask:0xf bank_mask:0xc\n"
      "s_waitcnt lgkmcnt(7)\n"
      "v_mfma_f32_16x16x32_bf16 a[16:19], v[96:99], a[132:135], a[16:19]\n"
      "v_perm_b32 v68, v5, v1, v235\n"
      "v_perm_b32 v233, v5, v1, v236\n"
      "v_mfma_f32_16x16x32_bf16 a[20:23], v[100:103], a[132:135], a[20:23]\n"
      "ds_read_b128 a[164:167], v221\n"
      "v_mfma_f32_16x16x32_bf16 a[24:27], v[104:107], a[132:135], a[24:27]\n"
      "global_load_dwordx4 v[36:39], v234, s[46:47] offset:-3072\n"
      "v_mfma_f32_16x16x32_bf16 a[28:31], v[108:111], a[132:135], a[28:31]\n"
      "v_cndmask_b32_dpp v84, v233, v68, vcc row_ror:8 row_mask:0xf bank_mask:0xf\n"
      "v_mov_b32_dpp v68, v233 row_ror:8 row_mask:0xf bank_mask:0xc\n"
      "s_waitcnt lgkmcnt(7)\n"
      "v_mfma_f32_16x16x32_bf16 a[32:35], v[96:99], a[136:139], a[32:35]\n"
      "v_perm_b32 v72, v6, v2, v235\n"
      "v_perm_b32 v232, v6, v2, v236\n"
      "v_mfma_f32_16x16x32_bf16 a[36:39], v[100:103], a[136:139], a[36:39]\n"
      "ds_read_b128 a[168:171], v222\n"
      "v_mfma_f32_16x16x32_bf16 a[40:43], v[104:107], a[136:139], a[40:43]\n"
      "global_load_dwordx4 v[40:43], v234, s[46:47] offset:-2048\n"
      "v_mfma_f32_16x16x32_bf16 a[44:47], v[108:111], a[136:139], a[44:47]\n"
      "v_cndmask_b32_dpp v88, v232, v72, vcc row_ror:8 row_mask:0xf bank_mask:0xf\n"
      "v_mov_b32_dpp v72, v232 row_ror:8 row_mask:0xf bank_mask:0xc\n"
      "s_waitcnt lgkmcnt(7)\n"
      "v_mfma_f32_16x16x32_bf16 a[48:51], v[96:99], a[140:143], a[48:51]\n"
      "v_perm_b32 v76, v7, v3, v235\n"
      "v_perm_b32 v233, v7, v3, v236\n"
      "v_mfma_f32_16x16x32_bf16 a[52:55], v[100:103], a[140:143], a[52:55]\n"
      "ds_read_b128 a[172:175], v223\n"
      "v_mfma_f32_16x16x32_bf16 a[56:59], v[104:107], a[140:143], a[56:59]\n"
      "global_load_dwordx4 v[44:47], v234, s[46:47] offset:-1024\n"
      "v_mfma_f32_16x16x32_bf16 a[60:63], v[108:111], a[140:143], a[60:63]\n"
      "v_cndmask_b32_dpp v92, v233, v76, vcc row_ror:8 row_mask:0xf bank_mask:0xf\n"
      "v_mov_b32_dpp v76, v233 row_ror:8 row_mask:0xf bank_mask:0xc\n"
      "s_waitcnt lgkmcnt(7)\n"
      "v_mfma_f32_16x16x32_bf16 a[64:67], v[96:99], a[144:147], a[64:67]\n"
      "s_waitcnt vmcnt(8)\n"
      "v_perm_b32 v65, v12, v8, v235\n"
      "v_perm_b32 v232, v12, v8, v236\n"
      "v_mfma_f32_16x16x32_bf16 a[68:71], v[100:103], a[144:147], a[68:71]\n"
      "ds_read_b128 a[176:179], v224\n"
      "v_mfma_f32_16x16x32_bf16 a[72:75], v[104:107], a[144:147], a[72:75]\n"
      "global_load_dwordx4 v[48:51], v234, s[46:47] offset:0\n"
      "v_mfma_f32_16x16x32_bf16 a[76:79], v[108:111], a[144:147], a[76:79]\n"
      "v_cndmask_b32_dpp v81, v232, v65, vcc row_ror:8 row_mask:0xf bank_mask:0xf\n"
      "v_mov_b32_dpp v65, v232 row_ror:8 row_mask:0xf bank_mask:0xc\n"
      "s_waitcnt lgkmcnt(7)\n"
      "v_mfma_f32_16x16x32_bf16 a[80:83], v[96:99], a[148:151], a[80:83]\n"
      "v_perm_b32 v69, v13, v9, v235\n"
      "v_perm_b32 v233, v13, v9, v236\n"
      "v_mfma_f32_16x16x32_bf16 a[84:87], v[100:103], a[148:151], a[84:87]\n"
      "ds_read_b128 a[180:183], v225\n"
      "v_mfma_f32_16x16x32_bf16 a[88:91], v[104:107], a[148:151], a[88:91]\n"
      "global_load_dwordx4 v[52:55], v234, s[46:47] offset:1024\n"
      "v_mfma_f32_16x16x32_bf16 a[92:95], v[108:111], a[148:151], a[92:95]\n"
      "v_cndmask_b32_dpp v85, v233, v69, vcc row_ror:8 row_mask:0xf bank_mask:0xf\n"
      "v_mov_b32_dpp v69, v233 row_ror:8 row_mask:0xf bank_mask:0xc\n"
      "s_waitcnt lgkmcnt(7)\n"
      "v_mfma_f32_16x16x32_bf16 a[96:99], v[96:99], a[152:155], a[96:99]\n"
      "v_perm_b32 v73, v14, v10, v235\n"
      "v_perm_b32 v232, v14, v10, v236\n"
      "v_mfma_f32_16x16x32_bf16 a[100:103], v[100:103], a[152:155], a[100:103]\n"
      "ds_read_b128 a[184:187], v226\n"
      "v_mfma_f32_16x16x32_bf16 a[104:107], v[104:107], a[152:155], a[104:107]\n"
      "global_load_dwordx4 v[56:59], v234, s[46:47] offset:2048\n"
      "v_mfma_f32_16x16x32_bf16 a[108:111], v[108:111], a[152:155], a[108:111]\n"
      "v_cndmask_b32_dpp v89, v232, v73, vcc row_ror:8 row_mask:0xf bank_mask:0xf\n"
      "v_mov_b32_dpp v73, v232 row_ror:8 row_mask:0xf bank_mask:0xc\n"
      "s_waitcnt lgkmcnt(7)\n"
      "v_mfma_f32_16x16x32_bf16 a[112:115], v[96:99], a[156:159], a[112:115]\n"
      "v_perm_b32 v77, v15, v11, v235\n"
      "v_perm_b32 v233, v15, v11, v236\n"
      "v_mfma_f32_16x16x32_bf16 a[116:119], v[100:103], a[156:159], a[116:119]\n"
      "ds_read_b128 a[188:191], v227\n"
      "v_mfma_f32_16x16x32_bf16 a[120:123], v[104:107], a[156:159], a[120:123]\n"
      "global_load_dwordx4 v[60:63], v234, s[46:47] offset:3072\n"
      "v_mfma_f32_16x16x32_bf16 a[124:127], v[108:111], a[156:159], a[124:127]\n"
      "v_cndmask_b32_dpp v93, v233, v77, vcc row_ror:8 row_mask:0xf bank_mask:0xf\n"
      "v_mov_b32_dpp v77, v233 row_ror:8 row_mask:0xf bank_mask:0xc\n"
      "s_waitcnt lgkmcnt(7)\n"
      "v_mfma_f32_16x16x32_bf16 a[0:3], v[112:115], a[160:163], a[0:3]\n"
      "s_waitcnt vmcnt(10)\n"
      "v_perm_b32 v66, v20, v16, v235\n"
      "v_perm_b32 v232, v20, v16, v236\n"
      "v_mfma_f32_16x16x32_bf16 a[4:7], v[116:119], a[160:163], a[4:7]\n"
      "ds_read_b128 a[128:131], v176\n"
      "v_mfma_f32_16x16x32_bf16 a[8:11], v[120:123], a[160:163], a[8:11]\n"
      "v_mfma_f32_16x16x32_bf16 a[12:15], v[124:127], a[160:163], a[12:15]\n"
      "v_cndmask_b32_dpp v82, v232, v66, vcc row_ror:8 row_mask:0xf bank_mask:0xf\n"
      "v_mov_b32_dpp v66, v232 row_ror:8 row_mask:0xf bank_mask:0xc\n"
      "s_waitcnt lgkmcnt(7)\n"
      "v_mfma_f32_16x16x32_bf16 a[16:19], v[112:115], a[164:167], a[16:19]\n"
      "v_perm_b32 v70, v21, v17, v235\n"
      "v_perm_b32 v233, v21, v17, v236\n"
      "v_mfma_f32_16x16x32_bf16 a[20:23], v[116:119], a[164:167], a[20:23]\n"
      "ds_read_b128 a[132:135], v177\n"
      "v_mfma_f32_16x16x32_bf16 a[24:27], v[120:123], a[164:167], a[24:27]\n"
      "v_mfma_f32_16x16x32_bf16 a[28:31], v[124:127], a[164:167], a[28:31]\n"
      "v_cndmask_b32_dpp v86, v233, v70, vcc row_ror:8 row_mask:0xf bank_mask:0xf\n"
      "v_mov_b32_dpp v70, v233 row_ror:8 row_mask:0xf bank_mask:0xc\n"
      "s_waitcnt lgkmcnt(7)\n"
      "v_mfma_f32_16x16x32_bf16 a[32:35], v[112:115], a[168:171], a[32:35]\n"
      "v_perm_b32 v74, v22, v18, v235\n"
      "v_perm_b32 v232, v22, v18, v236\n"
      "v_mfma_f32_16x16x32_bf16 a[36:39], v[116:119], a[168:171], a[36:39]\n"
      "ds_read_b128 a[136:139], v178\n"
      "v_mfma_f32_16x16x32_bf16 a[40:43], v[120:123], a[168:171], a[40:43]\n"
      "v_mfma_f32_16x16x32_bf16 a[44:47], v[124:127], a[168:171], a[44:47]\n"
      "v_cndmask_b32_dpp v90, v232, v74, vcc row_ror:8 row_mask:0xf bank_mask:0xf\n"
      "v_mov_b32_dpp v74, v232 row_ror:8 row_mask:0xf bank_mask:0xc\n"
      "s_waitcnt lgkmcnt(7)\n"
      "v_mfma_f32_16x16x32_bf16 a[48:51], v[112:115], a[172:175], a[48:51]\n"
      "v_perm_b32 v78, v23, v19, v235\n"
      "v_perm_b32 v233, v23, v19, v236\n"
      "v_mfma_f32_16x16x32_bf16 a[52:55], v[116:119], a[172:175], a[52:55]\n"
      "ds_read_b128 a[140:143], v179\n"
      "v_mfma_f32_16x16x32_bf16 a[56:59], v[120:123], a[172:175], a[56:59]\n"
      "v_mfma_f32_16x16x32_bf16 a[60:63], v[124:127], a[172:175], a[60:63]\n"
      "v_cndmask_b32_dpp v94, v233, v78, vcc row_ror:8 row_mask:0xf bank_mask:0xf\n"
      "v_mov_b32_dpp v78, v233 row_ror:8 row_mask:0xf bank_mask:0xc\n"
      "s_waitcnt lgkmcnt(7)\n"
      "v_mfma_f32_16x16x32_bf16 a[64:67], v[112:115], a[176:179], a[64:67]\n"
      "s_waitcnt vmcnt(8)\n"
      "v_perm_b32 v67, v28, v24, v235\n"
      "v_perm_b32 v232, v28, v24, v236\n"
      "v_mfma_f32_16x16x32_bf16 a[68:71], v[116:119], a[176:179], a[68:71]\n"
      "v_mfma_f32_16x16x32_bf16 a[72:75], v[120:123], a[176:179], a[72:75]\n"
      "v_mfma_f32_16x16x32_bf16 a[76:79], v[124:127], a[176:179], a[76:79]\n"
      "v_cndmask_b32_dpp v83, v232, v67, vcc row_ror:8 row_mask:0xf bank_mask:0xf\n"
      "v_mov_b32_dpp v67, v232 row_ror:8 row_mask:0xf bank_mask:0xc\n"
      "s_waitcnt lgkmcnt(6)\n"
      "v_mfma_f32_16x16x32_bf16 a[80:83], v[112:115], a[180:183], a[80:83]\n"
      "v_perm_b32 v71, v29, v25, v235\n"
      "v_perm_b32 v233, v29, v25, v236\n"
      "v_mfma_f32_16x16x32_bf16 a[84:87], v[116:119], a[180:183], a[84:87]\n"
      "v_mfma_f32_16x16x32_bf16 a[88:91], v[120:123], a[180:183], a[88:91]\n"
      "v_mfma_f32_16x16x32_bf16 a[92:95], v[124:127], a[180:183], a[92:95]\n"
      "v_cndmask_b32_dpp v87, v233, v71, vcc row_ror:8 row_mask:0xf bank_mask:0xf\n"
      "v_mov_b32_dpp v71, v233 row_ror:8 row_mask:0xf bank_mask:0xc\n"
      "s_waitcnt lgkmcnt(5)\n"
      "v_mfma_f32_16x16x32_bf16 a[96:99], v[112:115], a[184:187], a[96:99]\n"
      "v_perm_b32 v75, v30, v26, v235\n"
      "v_perm_b32 v232, v30, v26, v236\n"
      "v_mfma_f32_16x16x32_bf16 a[100:103], v[116:119], a[184:187], a[100:103]\n"
      "v_mfma_f32_16x16x32_bf16 a[104:107], v[120:123], a[184:187], a[104:107]\n"
      "v_mfma_f32_16x16x32_bf16 a[108:111], v[124:127], a[184:187], a[108:111]\n"
      "v_cndmask_b32_dpp v91, v232, v75, vcc row_ror:8 row_mask:0xf bank_mask:0xf\n"
      "v_mov_b32_dpp v75, v232 row_ror:8 row_mask:0xf bank_mask:0xc\n"
      "s_waitcnt lgkmcnt(4)\n"
      "v_mfma_f32_16x16x32_bf16 a[112:115], v[112:115], a[188:191], a[112:115]\n"
      "v_perm_b32 v79, v31, v27, v235\n"
      "v_perm_b32 v233, v31, v27, v236\n"
      "v_mfma_f32_16x16x32_bf16 a[116:119], v[116:119], a[188:191], a[116:119]\n"
      "v_mfma_f32_16x16x32_bf16 a[120:123], v[120:123], a[188:191], a[120:123]\n"
      "v_mfma_f32_16x16x32_bf16 a[124:127], v[124:127], a[188:191], a[124:127]\n"
      "v_cndmask_b32_dpp v95, v233, v79, vcc row_ror:8 row_mask:0xf bank_mask:0xf\n"
      "v_mov_b32_dpp v79, v233 row_ror:8 row_mask:0xf bank_mask:0xc\n"
      "s_waitcnt lgkmcnt(3)\n"
      "v_mfma_f32_16x16x32_bf16 a[0:3], v[64:67], a[128:131], a[0:3]\n"
      "v_mfma_f32_16x16x32_bf16 a[4:7], v[68:71], a[128:131], a[4:7]\n"
      "ds_read_b128 a[160:163], v228\n"
      "v_mfma_f32_16x16x32_bf16 a[8:11], v[72:75], a[128:131], a[8:11]\n"
      "v_mfma_f32_16x16x32_bf16 a[12:15], v[76:79], a[128:131], a[12:15]\n"
      "s_waitcnt lgkmcnt(3)\n"
      "v_mfma_f32_16x16x32_bf16 a[16:19], v[64:67], a[132:135], a[16:19]\n"
      "v_mfma_f32_16x16x32_bf16 a[20:23], v[68:71], a[132:135], a[20:23]\n"
      "ds_read_b128 a[164:167], v229\n"
      "v_mfma_f32_16x16x32_bf16 a[24:27], v[72:75], a[132:135], a[24:27]\n"
      "v_mfma_f32_16x16x32_bf16 a[28:31], v[76:79], a[132:135], a[28:31]\n"
      "s_waitcnt lgkmcnt(3)\n"
      "v_mfma_f32_16x16x32_bf16 a[32:35], v[64:67], a[136:139], a[32:35]\n"
      "v_mfma_f32_16x16x32_bf16 a[36:39], v[68:71], a[136:139], a[36:39]\n"
      "ds_read_b128 a[168:171], v230\n"
      "v_mfma_f32_16x16x32_bf16 a[40:43], v[72:75], a[136:139], a[40:43]\n"
      "v_mfma_f32_16x16x32_bf16 a[44:47], v[76:79], a[136:139], a[44:47]\n"
      "s_waitcnt lgkmcnt(3)\n"
      "v_mfma_f32_16x16x32_bf16 a[48:51], v[64:67], a[140:143], a[48:51]\n"
      "v_mfma_f32_16x16x32_bf16 a[52:55], v[68:71], a[140:143], a[52:55]\n"
      "ds_read_b128 a[172:175], v231\n"
      "v_mfma_f32_16x16x32_bf16 a[56:59], v[72:75], a[140:143], a[56:59]\n"
      "v_mfma_f32_16x16x32_bf16 a[60:63], v[76:79], a[140:143], a[60:63]\n"
      "s_waitcnt lgkmcnt(3)\n"
      "v_mfma_f32_16x16x32_bf16 a[0:3], v[80:83], a[160:163], a[0:3]\n"
      "v_mfma_f32_16x16x32_bf16 a[4:7], v[84:87], a[160:163], a[4:7]\n"
      "v_mfma_f32_16x16x32_bf16 a[8:11], v[88:91], a[160:163], a[8:11]\n"
      "v_mfma_f32_16x16x32_bf16 a[12:15], v[92:95], a[160:163], a[12:15]\n"
      "s_waitcnt lgkmcnt(2)\n"
      "v_mfma_f32_16x16x32_bf16 a[16:19], v[80:83], a[164:167], a[16:19]\n"
      "v_mfma_f32_16x16x32_bf16 a[20:23], v[84:87], a[164:167], a[20:23]\n"
      "v_mfma_f32_16x16x32_bf16 a[24:27], v[88:91], a[164:167], a[24:27]\n"
      "v_mfma_f32_16x16x32_bf16 a[28:31], v[92:95], a[164:167], a[28:31]\n"
      "s_waitcnt lgkmcnt(1)\n"
      "v_mfma_f32_16x16x32_bf16 a[32:35], v[80:83], a[168:171], a[32:35]\n"
      "v_mfma_f32_16x16x32_bf16 a[36:39], v[84:87], a[168:171], a[36:39]\n"
      "v_mfma_f32_16x16x32_bf16 a[40:43], v[88:91], a[168:171], a[40:43]\n"
      "v_mfma_f32_16x16x32_bf16 a[44:47], v[92:95], a[168:171], a[44:47]\n"
      "s_waitcnt lgkmcnt(0)\n"
      "v_mfma_f32_16x16x32_bf16 a[48:51], v[80:83], a[172:175], a[48:51]\n"
      "v_mfma_f32_16x16x32_bf16 a[52:55], v[84:87], a[172:175], a[52:55]\n"
      "v_mfma_f32_16x16x32_bf16 a[56:59], v[88:91], a[172:175], a[56:59]\n"
      "v_mfma_f32_16x16x32_bf16 a[60:63], v[92:95], a[172:175], a[60:63]\n"
      "s_mov_b64 s[40:41], s[46:47]\n"
      "s_add_u32 s44, s44, 65536\n"
      "s_addc_u32 s45, s45, 0\n"
      "s_barrier\n"
      "s_cmp_lg_u32 s48, 0\n"
      "s_cbranch_scc1 5f\n"
      "s_mov_b32 s48, 1\n"
      "s_add_u32 s49, s56, 128\n"
      "s_add_u32 s42, %[tab0], s49\n"
      "s_addc_u32 s43, %[tab1], 0\n"
      "s_mov_b32 m0, s57\n"
      "s_nop 0\n"
      "global_load_lds_dwordx4 v239, s[42:43]\n"
      "s_add_u32 m0, m0, 1024\n"
      "s_add_u32 s42, s42, 2048\n"
      "s_addc_u32 s43, s43, 0\n"
      "s_waitcnt vmcnt(7)\n"
      "v_perm_b32 v64, v36, v32, v235\n"
      "v_perm_b32 v232, v36, v32, v236\n"
      "global_load_lds_dwordx4 v239, s[42:43]\n"
      "s_add_u32 m0, m0, 1024\n"
      "s_add_u32 s42, s42, 2048\n"
      "s_addc_u32 s43, s43, 0\n"
      "v_perm_b32 v68, v37, v33, v235\n"
      "v_perm_b32 v233, v37, v33, v236\n"
      "v_cndmask_b32_dpp v80, v232, v64, vcc row_ror:8 row_mask:0xf bank_mask:0xf\n"
      "v_mov_b32_dpp v64, v232 row_ror:8 row_mask:0xf bank_mask:0xc\n"
      "global_load_lds_dwordx4 v239, s[42:43]\n"
      "s_add_u32 m0, m0, 1024\n"
      "s_add_u32 s42, s42, 2048\n"
      "s_addc_u32 s43, s43, 0\n"
      "v_perm_b32 v72, v38, v34, v235\n"
      "v_perm_b32 v232, v38, v34, v236\n"
      "v_cndmask_b32_dpp v84, v233, v68, vcc row_ror:8 row_mask:0xf bank_mask:0xf\n"
      "v_mov_b32_dpp v68, v233 row_ror:8 row_mask:0xf bank_mask:0xc\n"
      "global_load_lds_dwordx4 v239, s[42:43]\n"
      "s_add_u32 m0, m0, 1024\n"
      "s_add_u32 s42, s42, 2048\n"
      "s_addc_u32 s43, s43, 0\n"
      "v_perm_b32 v76, v39, v35, v235\n"
      "v_perm_b32 v233, v39, v35, v236\n"
      "v_cndmask_b32_dpp v88, v232, v72, vcc row_ror:8 row_mask:0xf bank_mask:0xf\n"
      "v_mov_b32_dpp v72, v232 row_ror:8 row_mask:0xf bank_mask:0xc\n"
      "global_load_lds_dwordx4 v239, s[42:43]\n"
      "s_add_u32 m0, m0, 1024\n"
      "s_add_u32 s42, s42, 2048\n"
      "s_addc_u32 s43, s43, 0\n"
      "s_waitcnt vmcnt(9)\n"
      "v_perm_b32 v65, v44, v40, v235\n"
      "v_perm_b32 v232, v44, v40, v236\n"
      "v_cndmask_b32_dpp v92, v233, v76, vcc row_ror:8 row_mask:0xf bank_mask:0xf\n"
      "v_mov_b32_dpp v76, v233 row_ror:8 row_mask:0xf bank_mask:0xc\n"
      "global_load_lds_dwordx4 v239, s[42:43]\n"
      "s_add_u32 m0, m0, 1024\n"
      "s_add_u32 s42, s42, 2048\n"
      "s_addc_u32 s43, s43, 0\n"
      "v_perm_b32 v69, v45, v41, v235\n"
      "v_perm_b32 v233, v45, v41, v236\n"
      "v_cndmask_b32_dpp v81, v232, v65, vcc row_ror:8 row_mask:0xf bank_mask:0xf\n"
      "v_mov_b32_dpp v65, v232 row_ror:8 row_mask:0xf bank_mask:0xc\n"
      "global_load_lds_dwordx4 v239, s[42:43]\n"
      "s_add_u32 m0, m0, 1024\n"
      "s_add_u32 s42, s42, 2048\n"
      "s_addc_u32 s43, s43, 0\n"
      "v_perm_b32 v73, v46, v42, v235\n"
      "v_perm_b32 v232, v46, v42, v236\n"
      "v_cndmask_b32_dpp v85, v233, v69, vcc row_ror:8 row_mask:0xf bank_mask:0xf\n"
      "v_mov_b32_dpp v69, v233 row_ror:8 row_mask:0xf bank_mask:0xc\n"
      "global_load_lds_dwordx4 v239, s[42:43]\n"
      "s_add_u32 m0, m0, 1024\n"
      "s_add_u32 s42, s42, 2048\n"
      "s_addc_u32 s43, s43, 0\n"
      "v_perm_b32 v77, v47, v43, v235\n"
      "v_perm_b32 v233, v47, v43, v236\n"
      "v_cndmask_b32_dpp v89, v232, v73, vcc row_ror:8 row_mask:0xf bank_mask:0xf\n"
      "v_mov_b32_dpp v73, v232 row_ror:8 row_mask:0xf bank_mask:0xc\n"
      "global_load_lds_dwordx4 v239, s[42:43]\n"
      "s_add_u32 m0, m0, 1024\n"
      "s_add_u32 s42, s42, 2048\n"
      "s_addc_u32 s43, s43, 0\n"
      "s_waitcnt vmcnt(11)\n"
      "v_perm_b32 v66, v52, v48, v235\n"
      "v_perm_b32 v232, v52, v48, v236\n"
      "v_cndmask_b32_dpp v93, v233, v77, vcc row_ror:8 row_mask:0xf bank_mask:0xf\n"
      "v_mov_b32_dpp v77, v233 row_ror:8 row_mask:0xf bank_mask:0xc\n"
      "global_load_lds_dwordx4 v239, s[42:43]\n"
      "s_add_u32 m0, m0, 1024\n"
      "s_add_u32 s42, s42, 2048\n"
      "s_addc_u32 s43, s43, 0\n"
      "v_perm_b32 v70, v53, v49, v235\n"
      "v_perm_b32 v233, v53, v49, v236\n"
      "v_cndmask_b32_dpp v82, v232, v66, vcc row_ror:8 row_mask:0xf bank_mask:0xf\n"
      "v_mov_b32_dpp v66, v232 row_ror:8 row_mask:0xf bank_mask:0xc\n"
      "global_load_lds_dwordx4 v239, s[42:43]\n"
      "s_add_u32 m0, m0, 1024\n"
      "s_add_u32 s42, s42, 2048\n"
      "s_addc_u32 s43, s43, 0\n"
      "v_perm_b32 v74, v54, v50, v235\n"
      "v_perm_b32 v232, v54, v50, v236\n"
      "v_cndmask_b32_dpp v86, v233, v70, vcc row_ror:8 row_mask:0xf bank_mask:0xf\n"
      "v_mov_b32_dpp v70, v233 row_ror:8 row_mask:0xf bank_mask:0xc\n"
      "global_load_lds_dwordx4 v239, s[42:43]\n"
      "s_add_u32 m0, m0, 1024\n"
      "s_add_u32 s42, s42, 2048\n"
      "s_addc_u32 s43, s43, 0\n"
      "v_perm_b32 v78, v55, v51, v235\n"
      "v_perm_b32 v233, v55, v51, v236\n"
      "v_cndmask_b32_dpp v90, v232, v74, vcc row_ror:8 row_mask:0xf bank_mask:0xf\n"
      "v_mov_b32_dpp v74, v232 row_ror:8 row_mask:0xf bank_mask:0xc\n"
      "global_load_lds_dwordx4 v239, s[42:43]\n"
      "s_add_u32 m0, m0, 1024\n"
      "s_add_u32 s42, s42, 2048\n"
      "s_addc_u32 s43, s43, 0\n"
      "s_waitcnt vmcnt(13)\n"
      "v_perm_b32 v67, v60, v56, v235\n"
      "v_perm_b32 v232, v60, v56, v236\n"
      "v_cndmask_b32_dpp v94, v233, v78, vcc row_ror:8 row_mask:0xf bank_mask:0xf\n"
      "v_mov_b32_dpp v78, v233 row_ror:8 row_mask:0xf bank_mask:0xc\n"
      "global_load_lds_dwordx4 v239, s[42:43]\n"
      "s_add_u32 m0, m0, 1024\n"
      "s_add_u32 s42, s42, 2048\n"
      "s_addc_u32 s43, s43, 0\n"
      "v_perm_b32 v71, v61, v57, v235\n"
      "v_perm_b32 v233, v61, v57, v236\n"
      "v_cndmask_b32_dpp v83, v232, v67, vcc row_ror:8 row_mask:0xf bank_mask:0xf\n"
      "v_mov_b32_dpp v67, v232 row_ror:8 row_mask:0xf bank_mask:0xc\n"
      "global_load_lds_dwordx4 v239, s[42:43]\n"
      "s_add_u32 m0, m0, 1024\n"
      "s_add_u32 s42, s42, 2048\n"
      "s_addc_u32 s43, s43, 0\n"
      "v_perm_b32 v75, v62, v58, v235\n"
      "v_perm_b32 v232, v62, v58, v236\n"
      "v_cndmask_b32_dpp v87, v233, v71, vcc row_ror:8 row_mask:0xf bank_mask:0xf\n"
      "v_mov_b32_dpp v71, v233 row_ror:8 row_mask:0xf bank_mask:0xc\n"
      "global_load_lds_dwordx4 v239, s[42:43]\n"
      "s_add_u32 m0, m0, 1024\n"
      "s_add_u32 s42, s42, 2048\n"
      "s_addc_u32 s43, s43, 0\n"
      "v_perm_b32 v79, v63, v59, v235\n"
      "v_perm_b32 v233, v63, v59, v236\n"
      "v_cndmask_b32_dpp v91, v232, v75, vcc row_ror:8 row_mask:0xf bank_mask:0xf\n"
      "v_mov_b32_dpp v75, v232 row_ror:8 row_mask:0xf bank_mask:0xc\n"
      "global_load_lds_dwordx4 v239, s[42:43]\n"
      "s_add_u32 m0, m0, 1024\n"
      "s_add_u32 s42, s42, 2048\n"
      "s_addc_u32 s43, s43, 0\n"
      "s_nop 1\n"
      "v_cndmask_b32_dpp v95, v233, v79, vcc row_ror:8 row_mask:0xf bank_mask:0xf\n"
      "v_mov_b32_dpp v79, v233 row_ror:8 row_mask:0xf bank_mask:0xc\n"
      "global_load_lds_dwordx4 v239, s[42:43]\n"
      "s_add_u32 m0, m0, 1024\n"
      "s_add_u32 s42, s42, 2048\n"
      "s_addc_u32 s43, s43, 0\n"
      "global_load_lds_dwordx4 v239, s[42:43]\n"
      "s_add_u32 m0, m0, 1024\n"
      "s_add_u32 s42, s42, 2048\n"
      "s_addc_u32 s43, s43, 0\n"
      "global_load_lds_dwordx4 v239, s[42:43]\n"
      "s_add_u32 m0, m0, 1024\n"
      "s_add_u32 s42, s42, 2048\n"
      "s_addc_u32 s43, s43, 0\n"
      "global_load_lds_dwordx4 v239, s[42:43]\n"
      "s_add_u32 m0, m0, 1024\n"
      "s_add_u32 s42, s42, 2048\n"
      "s_addc_u32 s43, s43, 0\n"
      "global_load_lds_dwordx4 v239, s[42:43]\n"
      "s_add_u32 m0, m0, 1024\n"
      "s_add_u32 s42, s42, 2048\n"
      "s_addc_u32 s43, s43, 0\n"
      "global_load_lds_dwordx4 v239, s[42:43]\n"
      "s_add_u32 m0, m0, 1024\n"
      "s_add_u32 s42, s42, 2048\n"
      "s_addc_u32 s43, s43, 0\n"
      "global_load_lds_dwordx4 v239, s[42:43]\n"
      "s_add_u32 m0, m0, 1024\n"
      "s_add_u32 s42, s42, 2048\n"
      "s_addc_u32 s43, s43, 0\n"
      "global_load_lds_dwordx4 v239, s[42:43]\n"
      "s_add_u32 m0, m0, 1024\n"
      "s_add_u32 s42, s42, 2048\n"
      "s_addc_u32 s43, s43, 0\n"
      "global_load_lds_dwordx4 v239, s[42:43]\n"
      "s_add_u32 m0, m0, 1024\n"
      "s_add_u32 s42, s42, 2048\n"
      "s_addc_u32 s43, s43, 0\n"
      "global_load_lds_dwordx4 v239, s[42:43]\n"
      "s_add_u32 m0, m0, 1024\n"
      "s_add_u32 s42, s42, 2048\n"
      "s_addc_u32 s43, s43, 0\n"
      "global_load_lds_dwordx4 v239, s[42:43]\n"
      "s_add_u32 m0, m0, 1024\n"
      "s_add_u32 s42, s42, 2048\n"
      "s_addc_u32 s43, s43, 0\n"
      "global_load_lds_dwordx4 v239, s[42:43]\n"
      "s_add_u32 m0, m0, 1024\n"
      "s_add_u32 s42, s42, 2048\n"
      "s_addc_u32 s43, s43, 0\n"
      "global_load_lds_dwordx4 v239, s[42:43]\n"
      "s_add_u32 m0, m0, 1024\n"
      "s_add_u32 s42, s42, 2048\n"
      "s_addc_u32 s43, s43, 0\n"
      "global_load_lds_dwordx4 v239, s[42:43]\n"
      "s_add_u32 m0, m0, 1024\n"
      "s_add_u32 s42, s42, 2048\n"
      "s_addc_u32 s43, s43, 0\n"
      "s_mov_b64 exec, 0xffff\n"
      "global_load_lds_dwordx4 v239, s[42:43]\n"
      "s_mov_b64 exec, -1\n"
      "s_add_u32 s40, s40, 131072\n"
      "s_addc_u32 s41, s41, 0\n"
      "global_load_dwordx4 v[32:35], v234, s[40:41] offset:-4096\n"
      "global_load_dwordx4 v[36:39], v234, s[40:41] offset:-3072\n"
      "global_load_dwordx4 v[40:43], v234, s[40:41] offset:-2048\n"
      "global_load_dwordx4 v[44:47], v234, s[40:41] offset:-1024\n"
      "global_load_dwordx4 v[48:51], v234, s[40:41] offset:0\n"
      "global_load_dwordx4 v[52:55], v234, s[40:41] offset:1024\n"
      "global_load_dwordx4 v[56:59], v234, s[40:41] offset:2048\n"
      "global_load_dwordx4 v[60:63], v234, s[40:41] offset:3072\n"
      "s_waitcnt vmcnt(8)\n"
      "s_branch 2b\n"
      "5:\n"
      "v_mbcnt_lo_u32_b32 v238, -1, 0\n"
      "v_mbcnt_hi_u32_b32 v238, -1, v238\n"
      "v_lshlrev_b32 v238, 4, v238\n"
      "s_add_u32 s49, %[w], 1\n"
      "s_and_b32 s49, s49, 3\n"
      "s_mul_i32 s49, s49, 24576\n"
      "s_add_u32 s49, s49, 0\n"
      "v_add_u32 v65, s49, v238\n"
      "s_add_u32 s49, %[w], 2\n"
      "s_and_b32 s49, s49, 3\n"
      "s_mul_i32 s49, s49, 24576\n"
      "s_add_u32 s49, s49, 8192\n"
      "v_add_u32 v66, s49, v238\n"
      "s_add_u32 s49, %[w], 3\n"
      "s_and_b32 s49, s49, 3\n"
      "s_mul_i32 s49, s49, 24576\n"
      "s_add_u32 s49, s49, 16384\n"
      "v_add_u32 v67, s49, v238\n"
      "s_nop 4\n"
      "ds_write_b128 v65, a[32:35] offset:0\n"
      "v_accvgpr_read_b32 v0, a0\n"
      "v_accvgpr_read_b32 v1, a1\n"
      "ds_write_b128 v65, a[36:39] offset:1024\n"
      "v_accvgpr_read_b32 v2, a2\n"
      "v_accvgpr_read_b32 v3, a3\n"
      "ds_write_b128 v65, a[40:43] offset:2048\n"
      "v_accvgpr_read_b32 v4, a4\n"
      "v_accvgpr_read_b32 v5, a5\n"
      "ds_write_b128 v65, a[44:47] offset:3072\n"
      "v_accvgpr_read_b32 v6, a6\n"
      "v_accvgpr_read_b32 v7, a7\n"
      "ds_write_b128 v65, a[48:51] offset:4096\n"
      "v_accvgpr_read_b32 v8, a8\n"
      "v_accvgpr_read_b32 v9, a9\n"
      "ds_write_b128 v65, a[52:55] offset:5120\n"
      "v_accvgpr_read_b32 v10, a10\n"
      "v_accvgpr_read_b32 v11, a11\n"
      "ds_write_b128 v65, a[56:59] offset:6144\n"
      "v_accvgpr_read_b32 v12, a12\n"
      "v_accvgpr_read_b32 v13, a13\n"
      "ds_write_b128 v65, a[60:63] offset:7168\n"
      "v_accvgpr_read_b32 v14, a14\n"
      "v_accvgpr_read_b32 v15, a15\n"
      "ds_write_b128 v66, a[64:67] offset:0\n"
      "v_accvgpr_read_b32 v16, a16\n"
      "v_accvgpr_read_b32 v17, a17\n"
      "ds_write_b128 v66, a[68:71] offset:1024\n"
      "v_accvgpr_read_b32 v18, a18\n"
      "v_accvgpr_read_b32 v19, a19\n"
      "ds_write_b128 v66, a[72:75] offset:2048\n"
      "v_accvgpr_read_b32 v20, a20\n"
      "v_accvgpr_read_b32 v21, a21\n"
      "ds_write_b128 v66, a[76:79] offset:3072\n"
      "v_accvgpr_read_b32 v22, a22\n"
      "v_accvgpr_read_b32 v23, a23\n"
      "ds_write_b128 v66, a[80:83] offset:4096\n"
      "v_accvgpr_read_b32 v24, a24\n"
      "v_accvgpr_read_b32 v25, a25\n"
      "ds_write_b128 v66, a[84:87] offset:5120\n"
      "v_accvgpr_read_b32 v26, a26\n"
      "v_accvgpr_read_b32 v27, a27\n"
      "ds_write_b128 v66, a[88:91] offset:6144\n"
      "v_accvgpr_read_b32 v28, a28\n"
      "v_accvgpr_read_b32 v29, a29\n"
      "ds_write_b128 v66, a[92:95] offset:7168\n"
      "v_accvgpr_read_b32 v30, a30\n"
      "v_accvgpr_read_b32 v31, a31\n"
      "ds_write_b128 v67, a[96:99] offset:0\n"
      "ds_write_b128 v67, a[100:103] offset:1024\n"
      "ds_write_b128 v67, a[104:107] offset:2048\n"
      "ds_write_b128 v67, a[108:111] offset:3072\n"
      "ds_write_b128 v67, a[112:115] offset:4096\n"
      "ds_write_b128 v67, a[116:119] offset:5120\n"
      "ds_write_b128 v67, a[120:123] offset:6144\n"
      "ds_write_b128 v67, a[124:127] offset:7168\n"
      "s_waitcnt vmcnt(0) lgkmcnt(0)\n"
      "9:\n"
      :
        "={v[0:3]}"(acc[0][0]), "={v[4:7]}"(acc[0][1]), "={v[8:11]}"(acc[0][2]), "={v[12:15]}"(acc[0][3]),
        "={v[16:19]}"(acc[1][0]), "={v[20:23]}"(acc[1][1]), "={v[24:27]}"(acc[1][2]), "={v[28:31]}"(acc[1][3])
      : [idx0] "s"((unsigned)pi), [idx1] "s"((unsigned)(pi >> 32)), [tab0] "s"((unsigned)pt), [tab1] "s"((unsigned)(pt >> 32)),
        [w10] "s"((unsigned)pw), [w11] "s"((unsigned)(pw >> 32)), [tab] "s"(pt), [w1] "s"(pw),
        [b1] "s"((unsigned long long)b1), [w2] "s"((unsigned long long)W2), [w] "s"(w), [ns] "s"(ns), [rb] "s"(rb),
        [gr] "s"((unsigned long long)(ws + WS_GRAN_OFF + rb * (BM * 16 * 8))), [op] "s"((unsigned long long)(out + rb * BM))
      : "memory", "vcc", "scc",
        "v32", "v33", "v34", "v35", "v36", "v37", "v38", "v39", "v40", "v41", "v42", "v43", "v44", "v45", "v46", "v47", "v48", "v49", "v50", "v51", "v52", "v53", "v54", "v55", "v56", "v57", "v58", "v59", "v60", "v61", "v62", "v63", "v64", "v65", "v66", "v67", "v68", "v69", "v70", "v71", "v72", "v73", "v74", "v75", "v76", "v77", "v78", "v79", "v80", "v81", "v82", "v83", "v84", "v85", "v86", "v87", "v88", "v89", "v90", "v91", "v92", "v93", "v94", "v95", "v96", "v97", "v98", "v99", "v100", "v101", "v102", "v103", "v104", "v105", "v106", "v107", "v108", "v109", "v110", "v111", "v112", "v113", "v114", "v115", "v116", "v117", "v118", "v119", "v120", "v121", "v122", "v123", "v124", "v125", "v126", "v127", "v128", "v129", "v130", "v131", "v132", "v133", "v134", "v135", "v136", "v137", "v138", "v139", "v140", "v141", "v142", "v143", "v144", "v145", "v146", "v147", "v148", "v149", "v150", "v151", "v152", "v153", "v154", "v155", "v156", "v157", "v158", "v159", "v160", "v161", "v162", "v163", "v164", "v165", "v166", "v167", "v168", "v169", "v170", "v171", "v172", "v173", "v174", "v175", "v176", "v177", "v178", "v179", "v180", "v181", "v182", "v183", "v184", "v185", "v186", "v187", "v188", "v189", "v190", "v191", "v192", "v193", "v194", "v195", "v196", "v197", "v198", "v199", "v200", "v201", "v202", "v203", "v204", "v205", "v206", "v207", "v208", "v209", "v210", "v211", "v212", "v213", "v214", "v215", "v216", "v217", "v218", "v219", "v220", "v221", "v222", "v223", "v224", "v225", "v226", "v227", "v228", "v229", "v230", "v231", "v232", "v233", "v234", "v235", "v236", "v237", "v238", "v239",
        "a0", "a1", "a2", "a3", "a4", "a5", "a6", "a7", "a8", "a9", "a10", "a11", "a12", "a13", "a14", "a15", "a16", "a17", "a18", "a19", "a20", "a21", "a22", "a23", "a24", "a25", "a26", "a27", "a28", "a29", "a30", "a31", "a32", "a33", "a34", "a35", "a36", "a37", "a38", "a39", "a40", "a41", "a42", "a43", "a44", "a45", "a46", "a47", "a48", "a49", "a50", "a51", "a52", "a53", "a54", "a55", "a56", "a57", "a58", "a59", "a60", "a61", "a62", "a63", "a64", "a65", "a66", "a67", "a68", "a69", "a70", "a71", "a72", "a73", "a74", "a75", "a76", "a77", "a78", "a79", "a80", "a81", "a82", "a83", "a84", "a85", "a86", "a87", "a88", "a89", "a90", "a91", "a92", "a93", "a94", "a95", "a96", "a97", "a98", "a99", "a100", "a101", "a102", "a103", "a104", "a105", "a106", "a107", "a108", "a109", "a110", "a111", "a112", "a113", "a114", "a115", "a116", "a117", "a118", "a119", "a120", "a121", "a122", "a123", "a124", "a125", "a126", "a127", "a128", "a129", "a130", "a131", "a132", "a133", "a134", "a135", "a136", "a137", "a138", "a139", "a140", "a141", "a142", "a143", "a144", "a145", "a146", "a147", "a148", "a149", "a150", "a151", "a152", "a153", "a154", "a155", "a156", "a157", "a158", "a159", "a160", "a161", "a162", "a163", "a164", "a165", "a166", "a167", "a168", "a169", "a170", "a171", "a172", "a173", "a174", "a175", "a176", "a177", "a178", "a179", "a180", "a181", "a182", "a183", "a184", "a185", "a186", "a187", "a188", "a189", "a190", "a191", "a192", "a193", "a194", "a195", "a196",
        "s40", "s41", "s42", "s43", "s44", "s45", "s46", "s47", "s48", "s49", "s50", "s51", "s52", "s53", "s54", "s55", "s56", "s57", "s58", "s59", "s60", "s61", "s62", "s63");
  }
  unsigned bp[4][2], wp[4][2];
  {
    u32x4 bq[4], wq[4];
#pragma unroll
    for (int r = 0; r < 4; ++r) {
      const int m7 = (4 * la + r) & 7;
      bq[r] = *(const LAS u32x4*)((const LAS char*)lds + LDS_SMALL + m7 * 16);
      wq[r] = *(const LAS u32x4*)((const LAS char*)lds + LDS_SMALL + 128 + m7 * 16);
    }
    const unsigned sel = (la >> 1) ? 0x07060302u : 0x05040100u;
#pragma unroll
    for (int t = 0; t < 4; ++t)
#pragma unroll
      for (int rp = 0; rp < 2; ++rp) {
        bp[t][rp] = __builtin_amdgcn_perm(bq[2 * rp + 1][t], bq[2 * rp][t], sel);
        wp[t][rp] = __builtin_amdgcn_perm(wq[2 * rp + 1][t], wq[2 * rp][t], sel);
      }
  }
  __builtin_amdgcn_sched_barrier(0);
  __builtin_amdgcn_s_barrier();
  {
    const LAS char* base = (const LAS char*)lds + lane * 16 + w * 24576;
    f32x4 o[3][2][4];
#pragma unroll
    for (int s = 0; s < 3; ++s)
#pragma unroll
      for (int i = 0; i < 2; ++i)
#pragma unroll
        for (int t = 0; t < 4; ++t) o[s][i][t] = *(const LAS f32x4*)(base + s * 8192 + i * 4096 + t * 1024);
    __builtin_amdgcn_sched_barrier(0);
#pragma unroll
    for (int i = 0; i < 2; ++i)
#pragma unroll
      for (int t = 0; t < 4; ++t) acc[i][t] = (acc[i][t] + o[0][i][t]) + (o[1][i][t] + o[2][i][t]);
  }
  float part[2] = {0.f, 0.f};
  {
#pragma unroll
    for (int t = 0; t < 4; ++t)
#pragma unroll
      for (int rp = 0; rp < 2; ++rp) {
        const f32x2 bb = {__uint_as_float(bp[t][rp] << 16), __uint_as_float(bp[t][rp] & 0xffff0000u)};
#pragma unroll
        for (int i = 0; i < 2; ++i) {
          const f32x2 a = {acc[i][t][2 * rp], acc[i][t][2 * rp + 1]};
          const unsigned g = __builtin_bit_cast(unsigned, __builtin_convertvector(a, bf16x2));
          const f32x2 s = f32x2{__uint_as_float(g << 16), __uint_as_float(g & 0xffff0000u)} + bb;
          s16x2 h = __builtin_bit_cast(s16x2, __builtin_convertvector(s, bf16x2));
          h = __builtin_elementwise_max(h, s16x2{0, 0});
          part[i] = __builtin_amdgcn_fdot2_f32_bf16(__builtin_bit_cast(bf16x2, h), __builtin_bit_cast(bf16x2, wp[t][rp]), part[i], false);
        }
      }
  }
#pragma unroll
  for (int i = 0; i < 2; ++i) {
    part[i] += __shfl_xor(part[i], 16);
    part[i] += __shfl_xor(part[i], 32);
  }
  const unsigned tag = epoch + 1u;
  gu64* gran = (gu64*)(ws + WS_GRAN_OFF) + (size_t)rb * (BM * 16);
  {
    const int tile = la >> 1, half = la & 1;
    const float v = tile ? part[1] : part[0];
    const int row = (2 * w + tile) * 16 + lb;
    const unsigned tg = half ? (tag ^ TAGB) : tag;
    __hip_atomic_store(gran + row * 16 + ns * 2 + half, ((unsigned long long)tg << 32) | __float_as_uint(v),
                       __ATOMIC_RELAXED, __HIP_MEMORY_SCOPE_AGENT);
  }
  if (w != 0) return;
  float wide = 0.f;
#pragma unroll
  for (int e = 0; e < 4; ++e)
    if (fq + 4 * e < NWIDE) wide += bf2f(xv[e]) * bf2f(wv[e]);
  wide += __shfl_xor(wide, 1);
  wide += __shfl_xor(wide, 2);
  wide = rbf(rbf(wide) + bf2f(bwv));
  const float bias2 = bf2f(b2v);
  gu64* gp = gran + ns * 256 + lane * 4;
  float s = 0.f;
  for (unsigned spins = 0; spins < (1u << 22); ++spins) {
    unsigned long long x0 = __hip_atomic_load(gp + 0, __ATOMIC_RELAXED, __HIP_MEMORY_SCOPE_AGENT);
    unsigned long long x1 = __hip_atomic_load(gp + 1, __ATOMIC_RELAXED, __HIP_MEMORY_SCOPE_AGENT);
    unsigned long long x2 = __hip_atomic_load(gp + 2, __ATOMIC_RELAXED, __HIP_MEMORY_SCOPE_AGENT);
    unsigned long long x3 = __hip_atomic_load(gp + 3, __ATOMIC_RELAXED, __HIP_MEMORY_SCOPE_AGENT);
    const bool ok = (unsigned)(x0 >> 32) == tag && (unsigned)(x1 >> 32) == (tag ^ TAGB) &&
                    (unsigned)(x2 >> 32) == tag && (unsigned)(x3 >> 32) == (tag ^ TAGB);
    s = __uint_as_float((unsigned)x0) + __uint_as_float((unsigned)x2);
    if (__all(ok)) break;
    __builtin_amdgcn_s_sleep(1);
  }
  s += __shfl_xor(s, 1);
  s += __shfl_xor(s, 2);
  const float deep = rbf(rbf(s) + bias2);
  const float z = rbf(wide + deep);
  const float y = rbf(1.f / (1.f + __expf(-z)));
  if (fq == 0) out[frow] = (unsigned short)(__float_as_uint(y) >> 16);
  if (ns == 0 && lane == 0)
    __hip_atomic_store((gu32*)(ws + rb * 256), tag, __ATOMIC_RELAXED, __HIP_MEMORY_SCOPE_AGENT);
}

extern "C" void kernel_launch(void* const* d_in, const int* in_sizes, int n_in, void* d_out, int out_size,
                              void* d_ws, size_t ws_size, hipStream_t stream) {
  (void)in_sizes; (void)n_in; (void)out_size; (void)ws_size;
  (void)hipFuncSetAttribute((const void*)wide_deep_kernel, hipFuncAttributeMaxDynamicSharedMemorySize, LDS_BYTES);
  wide_deep_kernel<<<256, 256, LDS_BYTES, stream>>>(
      (const int*)d_in[0], (const char*)d_in[1], (const unsigned short*)d_in[2], (const unsigned short*)d_in[3],
      (const unsigned short*)d_in[4], (const char*)d_in[5], (const unsigned short*)d_in[6],
      (const unsigned short*)d_in[7], (const unsigned short*)d_in[8], (unsigned short*)d_out, (char*)d_ws);
}
```
